```python
import torch

def _softmax(x, axis=-1):
    return torch.softmax(x, dim=axis)
def _gelu(x):
    return torch.nn.functional.gelu(x, approximate="tanh")
def _silu(x):
    return torch.nn.functional.silu(x)
def _sigmoid(x):
    return torch.sigmoid(x)
def _layernorm(x, axis=-1, eps=1e-5):
    m = x.mean(dim=axis, keepdim=True)
    v = x.var(dim=axis, keepdim=True, unbiased=False)
    return (x - m) / torch.sqrt(v + eps)
def _scatter_add(tgt, idx, src, axis=0):
    return tgt.clone().index_add_(axis, idx, src)
def _segment_sum(src, seg, n):
    out = torch.zeros((n,) + src.shape[1:], dtype=src.dtype)
    return out.index_add_(0, seg, src)
def _pad(x, pw):
    flat = tuple(v for pair in reversed(pw) for v in pair)
    return torch.nn.functional.pad(x, flat)

kernel_name = 'interleave_layers__spectral_fnet__dense_activation_relu__prompt'

def setup_inputs():

    _w = (lambda a: (lambda t: t.to(torch.bfloat16) if t.is_floating_point() else t)(torch.as_tensor(a)))
    def _A_setup():
        B, S, D = 1, 2048, 640
        ns = torch.arange(S)[:, None] * torch.arange(S)[None, :]
        nd = torch.arange(D)[:, None] * torch.arange(D)[None, :]
        Ws_re = torch.cos(-2.0 * torch.pi * ns / S).to(float)
        Ws_im = torch.sin(-2.0 * torch.pi * ns / S).to(float)
        Wd_re = torch.cos(-2.0 * torch.pi * nd / D).to(float)
        Wd_im = torch.sin(-2.0 * torch.pi * nd / D).to(float)
        return {
            "x": _w((lambda *s: torch.randn(*s, dtype=torch.bfloat16) * 0.05)(B, S, D)),
            "dft_seq_re": _w(Ws_re),
            "dft_seq_im": _w(Ws_im),
            "dft_hid_re": _w(Wd_re),
            "dft_hid_im": _w(Wd_im),
        }
    _A_in = _A_setup()

    def _B_setup():
        B, S, D, Dff = 1, 2048, 640, 1728
        return {
            "x": _w((lambda *s: torch.randn(*s, dtype=torch.bfloat16) * 0.05)(B, S, D)),
            "w1": _w((lambda *s: torch.randn(*s, dtype=torch.bfloat16) * 1.6)(D, Dff)),
            "w2": _w((lambda *s: torch.randn(*s, dtype=torch.bfloat16) * 1.6)(Dff, D)),
        }
    _B_in = _B_setup()

    return {
        "A_x": _A_in["x"],
        "A_dft_seq_re": _A_in["dft_seq_re"],
        "A_dft_seq_im": _A_in["dft_seq_im"],
        "A_dft_hid_re": _A_in["dft_hid_re"],
        "A_dft_hid_im": _A_in["dft_hid_im"],
        "B_x": _B_in["x"],
        "B_w1": _B_in["w1"],
        "B_w2": _B_in["w2"],
    }

def reference(A_x, A_dft_seq_re, A_dft_seq_im, A_dft_hid_re, A_dft_hid_im, B_x, B_w1, B_w2):
    def _A_ref(x, dft_seq_re, dft_seq_im, dft_hid_re, dft_hid_im):
        a = torch.einsum("bsd,ed->bse", x, dft_hid_re)
        b = torch.einsum("bsd,ed->bse", x, dft_hid_im)
        y = torch.einsum("ts,bse->bte", dft_seq_re, a) - torch.einsum("ts,bse->bte", dft_seq_im, b)
        return y

    def _B_ref(x, w1, w2):
        h = torch.einsum("bsd,df->bsf", x, w1)
        zero = torch.zeros_like(h)
        h = torch.maximum(h, zero)
        return torch.einsum("bsf,fd->bsd", h, w2)

    x = A_x
    x = x + _A_ref(_layernorm(x, axis=-1), A_dft_seq_re, A_dft_seq_im, A_dft_hid_re, A_dft_hid_im)
    x = x + _B_ref(_layernorm(x, axis=-1), B_w1, B_w2)
    x = x + _A_ref(_layernorm(x, axis=-1), A_dft_seq_re, A_dft_seq_im, A_dft_hid_re, A_dft_hid_im)
    x = x + _B_ref(_layernorm(x, axis=-1), B_w1, B_w2)
    return x
```

```cpp
#include <hip/hip_runtime.h>
#include <hip/hip_bf16.h>
#include <stdint.h>
typedef unsigned short u16;
using bf16x8 = __attribute__((ext_vector_type(8))) short;
using f32x4  = __attribute__((ext_vector_type(4))) float;
static __device__ __forceinline__ float bf2f(u16 b){ return __uint_as_float(((unsigned)b)<<16); }
static __device__ __forceinline__ u16 f2bf(float f){ __hip_bfloat16 h = __float2bfloat16(f); return *(u16*)&h; }

constexpr int S=2048, D=640, F=1728;

__global__ void ln_kernel(const u16* __restrict__ x, u16* __restrict__ xn){
  int row = blockIdx.x*4 + (threadIdx.x>>6); int lane = threadIdx.x&63;
  float v[10]; float s=0;
  for(int i=0;i<10;i++){ v[i]=bf2f(x[row*D+lane+64*i]); s+=v[i]; }
  for(int o=32;o>0;o>>=1) s+=__shfl_xor(s,o);
  float m=s/D; float q=0;
  for(int i=0;i<10;i++){ float d=v[i]-m; q+=d*d; }
  for(int o=32;o>0;o>>=1) q+=__shfl_xor(q,o);
  float r=rsqrtf(q/D+1e-5f);
  for(int i=0;i<10;i++) xn[row*D+lane+64*i]=f2bf((v[i]-m)*r);
}

template<int EPI, bool TWO>
__global__ __launch_bounds__(256) void gemm_k(const u16* __restrict__ A, int lda, const u16* __restrict__ B, int sbk, int sbn,
   const u16* __restrict__ A2, const u16* __restrict__ B2, u16* __restrict__ C, int scm, int scn, const u16* __restrict__ R, int K){
  __shared__ u16 sA[64][40];
  __shared__ u16 sB[64][40];
  int bm=blockIdx.y*64, bn=blockIdx.x*64;
  int tid=threadIdx.x, w=tid>>6, lane=tid&63, wr=w>>1, wc=w&1, fr=lane&15, fq=lane>>4;
  f32x4 acc[2][2]={}; f32x4 acc2[2][2]={};
  for(int pass=0; pass<(TWO?2:1); pass++){
    const u16* Ap = pass? A2:A; const u16* Bp = pass? B2:B;
    for(int k0=0;k0<K;k0+=32){
      for(int e=tid;e<64*32;e+=256){ int r=e>>5,c=e&31; sA[r][c]=Ap[(size_t)(bm+r)*lda+k0+c]; }
      if(sbk==1){ for(int e=tid;e<64*32;e+=256){ int r=e>>5,c=e&31; sB[r][c]=Bp[(size_t)(bn+r)*sbn+k0+c]; } }
      else { for(int e=tid;e<64*32;e+=256){ int r=e&63,c=e>>6; sB[r][c]=Bp[(size_t)(k0+c)*sbk+bn+r]; } }
      __syncthreads();
      bf16x8 a[2],b[2];
      for(int i=0;i<2;i++){ a[i]=*(const bf16x8*)&sA[wr*32+i*16+fr][fq*8]; b[i]=*(const bf16x8*)&sB[wc*32+i*16+fr][fq*8]; }
      for(int i=0;i<2;i++)for(int j=0;j<2;j++){
        if(pass==0) acc[i][j]=__builtin_amdgcn_mfma_f32_16x16x32_bf16(a[i],b[j],acc[i][j],0,0,0);
        else acc2[i][j]=__builtin_amdgcn_mfma_f32_16x16x32_bf16(a[i],b[j],acc2[i][j],0,0,0);
      }
      __syncthreads();
    }
  }
  for(int i=0;i<2;i++)for(int j=0;j<2;j++)for(int r=0;r<4;r++){
    int m=bm+wr*32+i*16+fq*4+r, n=bn+wc*32+j*16+fr;
    float v=acc[i][j][r]; if(TWO) v-=acc2[i][j][r];
    if(EPI==1) v=fmaxf(v,0.f);
    if(EPI==2) v+=bf2f(R[m*D+n]);
    C[(size_t)m*scm+(size_t)n*scn]=f2bf(v);
  }
}

extern "C" void kernel_launch(void* const* d_in, const int* in_sizes, int n_in, void* d_out, int out_size, void* d_ws, size_t ws_size, hipStream_t stream){
  const u16* x0=(const u16*)d_in[0]; const u16* Cre=(const u16*)d_in[1]; const u16* Cim=(const u16*)d_in[2];
  const u16* Wre=(const u16*)d_in[3]; const u16* Wim=(const u16*)d_in[4]; const u16* w1=(const u16*)d_in[6]; const u16* w2=(const u16*)d_in[7];
  u16* ws=(u16*)d_ws; u16* xn=ws; u16* aT=xn+S*D; u16* bT=aT+S*D; u16* x1=bT+S*D; u16* h=x1+S*D; u16* x2=h+S*F;
  const u16* xin=x0;
  for(int l=0;l<2;l++){
    u16* xo = l==0? x2 : (u16*)d_out;
    ln_kernel<<<S/4,256,0,stream>>>(xin,xn);
    gemm_k<0,false><<<dim3(D/64,S/64),256,0,stream>>>(xn,D,Wre,1,D,nullptr,nullptr,aT,1,S,nullptr,D);
    gemm_k<0,false><<<dim3(D/64,S/64),256,0,stream>>>(xn,D,Wim,1,D,nullptr,nullptr,bT,1,S,nullptr,D);
    gemm_k<2,true><<<dim3(D/64,S/64),256,0,stream>>>(Cre,S,aT,1,S,Cim,bT,x1,D,1,xin,S);
    ln_kernel<<<S/4,256,0,stream>>>(x1,xn);
    gemm_k<1,false><<<dim3(F/64,S/64),256,0,stream>>>(xn,D,w1,F,1,nullptr,nullptr,h,F,1,nullptr,D);
    gemm_k<2,false><<<dim3(D/64,S/64),256,0,stream>>>(h,F,w2,D,1,nullptr,nullptr,xo,D,1,x1,F);
    xin=x2;
  }
}
```

```cpp
#include <hip/hip_runtime.h>
#include <hip/hip_bf16.h>
#include <stdint.h>
typedef unsigned short u16;
using bf16x8 = __attribute__((ext_vector_type(8))) short;
using f32x4  = __attribute__((ext_vector_type(4))) float;
static __device__ __forceinline__ float bf2f(u16 b){ return __uint_as_float(((unsigned)b)<<16); }
static __device__ __forceinline__ u16 f2bf(float f){ __hip_bfloat16 h = __float2bfloat16(f); return *(u16*)&h; }

constexpr int S=2048, D=640, F=1728;

__global__ void ln_kernel(const u16* __restrict__ x, u16* __restrict__ xn){
  int row = blockIdx.x*4 + (threadIdx.x>>6); int lane = threadIdx.x&63;
  float v[10]; float s=0;
  for(int i=0;i<10;i++){ v[i]=bf2f(x[row*D+lane+64*i]); s+=v[i]; }
  for(int o=32;o>0;o>>=1) s+=__shfl_xor(s,o);
  float m=s/D; float q=0;
  for(int i=0;i<10;i++){ float d=v[i]-m; q+=d*d; }
  for(int o=32;o>0;o>>=1) q+=__shfl_xor(q,o);
  float r=rsqrtf(q/D+1e-5f);
  for(int i=0;i<10;i++) xn[row*D+lane+64*i]=f2bf((v[i]-m)*r);
}

template<int EPI, bool TWO>
__global__ __launch_bounds__(256) void gemm_k(const u16* __restrict__ A, int lda, const u16* __restrict__ B, int sbk, int sbn,
   const u16* __restrict__ A2, const u16* __restrict__ B2, u16* __restrict__ C, int scm, int scn, const u16* __restrict__ R, int K){
  __shared__ u16 sA[64][40];
  __shared__ u16 sB[64][40];
  int bm=blockIdx.y*64, bn=blockIdx.x*64;
  int tid=threadIdx.x, w=tid>>6, lane=tid&63, wr=w>>1, wc=w&1, fr=lane&15, fq=lane>>4;
  f32x4 acc[2][2]={}; f32x4 acc2[2][2]={};
  for(int pass=0; pass<(TWO?2:1); pass++){
    const u16* Ap = pass? A2:A; const u16* Bp = pass? B2:B;
    for(int k0=0;k0<K;k0+=32){
      for(int e=tid;e<64*32;e+=256){ int r=e>>5,c=e&31; sA[r][c]=Ap[(size_t)(bm+r)*lda+k0+c]; }
      if(sbk==1){ for(int e=tid;e<64*32;e+=256){ int r=e>>5,c=e&31; sB[r][c]=Bp[(size_t)(bn+r)*sbn+k0+c]; } }
      else { for(int e=tid;e<64*32;e+=256){ int r=e&63,c=e>>6; sB[r][c]=Bp[(size_t)(k0+c)*sbk+bn+r]; } }
      __syncthreads();
      bf16x8 a[2],b[2];
      for(int i=0;i<2;i++){ a[i]=*(const bf16x8*)&sA[wr*32+i*16+fr][fq*8]; b[i]=*(const bf16x8*)&sB[wc*32+i*16+fr][fq*8]; }
      for(int i=0;i<2;i++)for(int j=0;j<2;j++){
        if(pass==0) acc[i][j]=__builtin_amdgcn_mfma_f32_16x16x32_bf16(a[i],b[j],acc[i][j],0,0,0);
        else acc2[i][j]=__builtin_amdgcn_mfma_f32_16x16x32_bf16(a[i],b[j],acc2[i][j],0,0,0);
      }
      __syncthreads();
    }
  }
  for(int i=0;i<2;i++)for(int j=0;j<2;j++)for(int r=0;r<4;r++){
    int m=bm+wr*32+i*16+fq*4+r, n=bn+wc*32+j*16+fr;
    float v=acc[i][j][r]; if(TWO) v-=acc2[i][j][r];
    if(EPI==1) v=fmaxf(v,0.f);
    if(EPI==2) v+=bf2f(R[m*D+n]);
    C[(size_t)m*scm+(size_t)n*scn]=f2bf(v);
  }
}


typedef unsigned v4u __attribute__((ext_vector_type(4)));
#define GAS __attribute__((address_space(1)))
template<int NL>
__global__ __launch_bounds__(512) void probe_k(const char* p0,const char* p1,unsigned* sink,int amp){
  const int tid=threadIdx.x,bid=blockIdx.x;
  unsigned long long t0=__builtin_amdgcn_s_memrealtime();
  v4u r[2*NL]; unsigned x=0;
  #pragma unroll
  for(int i=0;i<NL;i++){ r[i]=*(const GAS v4u*)(p0+((size_t)bid*NL+i)*8192+tid*16); r[NL+i]=*(const GAS v4u*)(p1+((size_t)bid*NL+i)*8192+tid*16); }
  #pragma unroll
  for(int i=0;i<2*NL;i++) x^=r[i].x^r[i].y^r[i].z^r[i].w;
  if(x==0x12345678) sink[tid]=x;
  __syncthreads();
  unsigned long long t1=__builtin_amdgcn_s_memrealtime();
  unsigned long long until=t1+(t1-t0)*amp;
  while(__builtin_amdgcn_s_memrealtime()<until) __builtin_amdgcn_s_sleep(4);
}

extern "C" void kernel_launch(void* const* d_in, const int* in_sizes, int n_in, void* d_out, int out_size, void* d_ws, size_t ws_size, hipStream_t stream){
  const u16* x0=(const u16*)d_in[0]; const u16* Cre=(const u16*)d_in[1]; const u16* Cim=(const u16*)d_in[2];
  const u16* Wre=(const u16*)d_in[3]; const u16* Wim=(const u16*)d_in[4]; const u16* w1=(const u16*)d_in[6]; const u16* w2=(const u16*)d_in[7];
  u16* ws=(u16*)d_ws; u16* xn=ws; u16* aT=xn+S*D; u16* bT=aT+S*D; u16* x1=bT+S*D; u16* h=x1+S*D; u16* x2=h+S*F;
  const u16* xin=x0;
  probe_k<4><<<256,512,0,stream>>>((const char*)Cre,(const char*)Cim,(unsigned*)((char*)d_ws+(200<<20)),0);
  for(int l=0;l<2;l++){
    u16* xo = l==0? x2 : (u16*)d_out;
    ln_kernel<<<S/4,256,0,stream>>>(xin,xn);
    gemm_k<0,false><<<dim3(D/64,S/64),256,0,stream>>>(xn,D,Wre,1,D,nullptr,nullptr,aT,1,S,nullptr,D);
    gemm_k<0,false><<<dim3(D/64,S/64),256,0,stream>>>(xn,D,Wim,1,D,nullptr,nullptr,bT,1,S,nullptr,D);
    gemm_k<2,true><<<dim3(D/64,S/64),256,0,stream>>>(Cre,S,aT,1,S,Cim,bT,x1,D,1,xin,S);
    ln_kernel<<<S/4,256,0,stream>>>(x1,xn);
    gemm_k<1,false><<<dim3(F/64,S/64),256,0,stream>>>(xn,D,w1,F,1,nullptr,nullptr,h,F,1,nullptr,D);
    gemm_k<2,false><<<dim3(D/64,S/64),256,0,stream>>>(h,F,w2,D,1,nullptr,nullptr,xo,D,1,x1,F);
    xin=x2;
  }
}
```

```cpp
#include <hip/hip_runtime.h>
#include <stdint.h>

typedef unsigned short u16;
typedef unsigned long long u64;
typedef float f32x2 __attribute__((ext_vector_type(2)));
typedef float f32x4 __attribute__((ext_vector_type(4)));
typedef float f32x8 __attribute__((ext_vector_type(8)));
typedef float f32x16 __attribute__((ext_vector_type(16)));
typedef unsigned v4u __attribute__((ext_vector_type(4)));
typedef unsigned v2u __attribute__((ext_vector_type(2)));
typedef unsigned v8u __attribute__((ext_vector_type(8)));
typedef unsigned v16u __attribute__((ext_vector_type(16)));
typedef __bf16 bf16x2 __attribute__((ext_vector_type(2)));
typedef __amdgpu_buffer_rsrc_t rsrc_t;
#define GAS __attribute__((address_space(1)))
#define LAS __attribute__((address_space(3)))
#define DI __device__ __forceinline__
#define ACLOB "a0","a1","a2","a3","a4","a5","a6","a7","a8","a9","a10","a11","a12","a13","a14","a15","a16","a17","a18","a19","a20","a21","a22","a23","a24","a25","a26","a27","a28","a29","a30","a31","a32","a33","a34","a35","a36","a37","a38","a39","a40","a41","a42","a43","a44","a45","a46","a47","a48","a49","a50","a51","a52","a53","a54","a55","a56","a57","a58","a59","a60","a61","a62","a63","a64","a65","a66","a67","a68","a69","a70","a71","a72","a73","a74","a75","a76","a77","a78","a79","a80","a81","a82","a83","a84","a85","a86","a87","a88","a89","a90","a91","a92","a93","a94","a95","a96","a97","a98","a99","a100","a101","a102","a103","a104","a105","a106","a107","a108","a109","a110","a111","a112","a113","a114","a115","a116","a117","a118","a119","a120","a121","a122","a123","a124","a125","a126","a127","a128","a129","a130","a131","a132","a133","a134","a135","a136","a137","a138","a139","a140","a141","a142","a143","a144","a145","a146","a147","a148","a149","a150","a151","a152","a153","a154","a155","a156","a157","a158","a159","a160","a161","a162","a163","a164","a165","a166","a167","a168","a169","a170","a171","a172","a173","a174","a175","a176","a177","a178","a179","a180","a181","a182","a183","a184","a185","a186","a187","a188","a189","a190","a191","a192","a193","a194","a195","a196","a197","a198","a199","a200","a201","a202","a203","a204","a205","a206","a207","a208","a209","a210","a211","a212","a213","a214","a215","a216","a217","a218","a219","a220","a221","a222","a223","a224","a225","a226","a227","a228","a229","a230","a231","a232","a233","a234","a235","a236","a237","a238","a239","a240","a241","a242","a243","a244","a245","a246","a247","a248","a249","a250","a251","a252","a253","a254","a255"


constexpr unsigned XU_B = 64u * 80u * 512u;
constexpr unsigned ABT_B = 40u * 256u * 512u;
constexpr unsigned H_B = 64u * 216u * 512u;
constexpr unsigned STA_B = 8u * 2048u * 8u, ST1_B = 20u * 2048u * 8u;
constexpr unsigned W1T_SL = 20u * 7u * 1024u, W2T_SL = 54u * 5u * 1024u;
constexpr unsigned OFF_E = 0, OFF_INIT = 4096, OFF_FL = 8192;
constexpr unsigned FL_X = 0, FL_AB = 128, FL_P = 256, FL_XA = 768, FL_XB = 896, FL_H = 1024;
constexpr unsigned OFF_CS = 640u * 1024u;
constexpr unsigned OFF_XA = 2u << 20;
constexpr unsigned OFF_X1 = OFF_XA + 2 * XU_B;
constexpr unsigned OFF_PART = OFF_X1 + 2 * XU_B;
constexpr unsigned OFF_ABT = OFF_PART + 8 * XU_B;
constexpr unsigned OFF_H = OFF_ABT + 2 * ABT_B;
constexpr unsigned OFF_STA = OFF_H + 2 * H_B;
constexpr unsigned OFF_ST1 = OFF_STA + 2 * STA_B;
constexpr unsigned OFF_W1T = OFF_ST1 + 2 * ST1_B;
constexpr unsigned OFF_W2T = OFF_W1T + 16 * W1T_SL;
constexpr unsigned WS_USED = OFF_W2T + 8 * W2T_SL;
constexpr unsigned L_PT = 131072;
constexpr unsigned L_CSD = 153600, L_CS1 = 154112, L_SW = 154624;
constexpr unsigned LDS_BYTES = 163840;
constexpr unsigned OOB = 0x80000000u;
constexpr int SC1 = 16;

DI unsigned rfl(unsigned x) { return __builtin_amdgcn_readfirstlane(x); }
DI unsigned cvtpk(float lo, float hi) { f32x2 v = {lo, hi}; bf16x2 b = __builtin_convertvector(v, bf16x2); return __builtin_bit_cast(unsigned, b); }
DI float bflo(unsigned u) { return __uint_as_float(u << 16); }
DI float bfhi(unsigned u) { return __uint_as_float(u & 0xffff0000u); }
DI int sigma(int s) { return ((s >> 2) & 3) * 8 + (s >> 4) * 4 + (s & 3); }
DI float sum_x8(float x) { return x + __uint_as_float(__builtin_amdgcn_update_dpp(0u, __float_as_uint(x), 0x128, 0xf, 0xf, false)); }
DI float sum_x16(float x) { auto p = __builtin_amdgcn_permlane16_swap(__float_as_uint(x), __float_as_uint(x), false, false); return __uint_as_float(p[0]) + __uint_as_float(p[1]); }
DI float sum_x32(float x) { auto p = __builtin_amdgcn_permlane32_swap(__float_as_uint(x), __float_as_uint(x), false, false); return __uint_as_float(p[0]) + __uint_as_float(p[1]); }
DI void bar() { asm volatile("s_waitcnt lgkmcnt(0)\n\ts_barrier" ::: "memory"); }
template <int N> DI void waitv() { asm volatile("s_waitcnt vmcnt(%0)" ::"n"(N) : "memory"); }
DI void dma(unsigned lds, unsigned voff, rsrc_t r, unsigned soff) {
  asm volatile("s_mov_b32 m0, %0\n\ts_nop 0\n\tbuffer_load_dwordx4 %1, %2, %3 offen lds" ::"s"(lds), "v"(voff), "s"(r), "s"(soff) : "memory");
}
DI void dma_sc1(unsigned lds, unsigned voff, rsrc_t r, unsigned soff) {
  asm volatile("s_mov_b32 m0, %0\n\ts_nop 0\n\tbuffer_load_dwordx4 %1, %2, %3 offen sc1 lds" ::"s"(lds), "v"(voff), "s"(r), "s"(soff) : "memory");
}
DI void st128(v4u d, rsrc_t r, unsigned voff, unsigned soff) {
  asm volatile("buffer_store_dwordx4 %0, %1, %2, %3 offen sc1\n\ts_nop 1" ::"v"(d), "v"(voff), "s"(r), "s"(soff) : "memory");
}
DI unsigned fline(int ly, unsigned type, int id) { return OFF_FL + ((unsigned)ly * 2048u + type + (unsigned)id) * 128u; }
DI void poll_issue(v2u& v, unsigned voff, rsrc_t rs, unsigned line) {
  asm volatile("buffer_load_dwordx2 %0, %1, %2, %3 offen sc1" : "=v"(v) : "v"(voff), "s"(rs), "s"(line) : "memory");
}
template <int N> DI bool poll_check(v2u& v, v2u ev) {
  asm volatile("s_waitcnt vmcnt(%1)" : "+v"(v) : "n"(N) : "memory");
  return __all(v.x == ev.x && v.y == ev.y);
}
DI void poll_spin(unsigned voff, rsrc_t rs, unsigned line, v2u ev) {
#pragma unroll 1
  for (int i = 0; i < 0x40000; ++i) { v2u v; poll_issue(v, voff, rs, line); if (poll_check<0>(v, ev)) break; }
}
DI void lin_pieces(int kb0, int n, int last, unsigned lane16, rsrc_t rs, unsigned src) {
#pragma unroll 1
  for (int i = 0; i < n; ++i) { const int kb = min(kb0 + 4 * i, last); dma_sc1(kb * 1024, lane16, rs, src + kb * 1024); }
}
DI void wd_groups(int n0, int n1, int w, int g0, unsigned vo, rsrc_t rWd) {
#pragma unroll 1
  for (int n = n0; n < n1; ++n) {
    const int G = g0 + n;
    const unsigned so = (unsigned)(32 * (G >> 1) + 4 * (G & 1) + 16 * (w & 1)) * 1280u + (w >> 1) * 640, sm = (w >> 1) * 51200 + (2 * n + (w & 1)) * 1024;
    asm volatile("s_add_u32 m0, %[sm], 0\n\t"
"s_nop 0\n\t"
"buffer_load_dwordx4 %[vo], %[rs], %[so] offen offset:0 lds\n\t"
"s_add_u32 m0, %[sm], 10112\n\t"
"s_nop 0\n\t"
"buffer_load_dwordx4 %[vo], %[rs], %[so] offen offset:128 lds\n\t"
"s_add_u32 m0, %[sm], 20224\n\t"
"s_nop 0\n\t"
"buffer_load_dwordx4 %[vo], %[rs], %[so] offen offset:256 lds\n\t"
"s_add_u32 m0, %[sm], 30336\n\t"
"s_nop 0\n\t"
"buffer_load_dwordx4 %[vo], %[rs], %[so] offen offset:384 lds\n\t"
"s_add_u32 m0, %[sm], 40448\n\t"
"s_nop 0\n\t"
"buffer_load_dwordx4 %[vo], %[rs], %[so] offen offset:512 lds\n\t"
 ::[sm] "s"(sm), [vo] "v"(vo), [rs] "s"(rWd), [so] "s"(so) : "memory", "scc");
  }
}

struct Params {
  const u16 *x0, *cre, *cim, *wre, *wim, *w1, *w2;
  u16* out;
  char* ws;
};

__global__ __launch_bounds__(256, 1) void fnet_fused(Params p) {
  extern __shared__ __attribute__((aligned(16))) char smem[];
  LAS char* L = (LAS char*)smem;
  const int tid = threadIdx.x, bid = blockIdx.x;
  const int w0 = rfl(tid >> 6);
#define PHASE_VARS()                                                                                     \
  int tq_ = tid, bq_ = bid, w = w0;                                                                      \
  asm volatile("" : "+v"(tq_));                                                                          \
  asm volatile("" : "+s"(bq_), "+s"(w));                                                                 \
  const int lane = tq_ & 63, xcd = bq_ & 7, bi = bq_ >> 3;                                               \
  const int rb = 4 * (xcd >> 1) + (bi >> 3), c16 = 8 * (xcd & 1) + (bi & 7);   \
  const int ks = xcd >> 1, rb2 = 8 * (xcd & 1) + (bi >> 2), cb = bi & 3;           \
  const int q = lane >> 4, r = lane & 15, hh = lane >> 5, j32 = lane & 31, half = c16 >> 3, half2 = xcd & 1; (void)half2;             \
  const unsigned lane16 = lane * 16;                                                                     \
  (void)rb; (void)c16; (void)ks; (void)rb2; (void)cb; (void)q; (void)r; (void)hh; (void)j32; (void)lane16; (void)half
#define PIECE_VARS()                      \
  const int g = xcd * 32 + bi;                                                                           \
  const bool isw1 = g < 108, haspiece = g < 218;                                                         \
  const int gg = isw1 ? g : g - 108;                                                                     \
  const int cblk = isw1 ? (gg >> 2) : (gg / 11), kq = isw1 ? (gg & 3) : (gg % 11)
#define A1_FO (fline(ly, FL_AB, (((2 * half + (rb >> 3)) * 4 + ((c16 & 7) >> 1)) * 4 + 2 * (rb & 1) + (w >> 1)) * 2) + (((rb & 7) >> 1) * 4 + (w & 1) * 2 + (c16 & 1)) * 8)
#define WD_VARS()                                                                                                                          \
  const rsrc_t rWd = __builtin_amdgcn_make_buffer_rsrc((void*)(half ? p.wim : p.wre), 0, 640 * 640 * 2, 0x00020000);                       \
  const unsigned wdrow = (unsigned)(8 * (lane >> 5) + ((lane >> 3) & 3)) * 1280u;                                         \
  const unsigned wdv = wdrow + (unsigned)(((lane & 7) ^ (lane >> 4) ^ (4 * (w & 1))) * 16)
  const rsrc_t rs = __builtin_amdgcn_make_buffer_rsrc((void*)p.ws, 0, WS_USED, 0x00020000);
  unsigned ep0, ep1;

  {
    PHASE_VARS();
    WD_VARS();
    const rsrc_t rX = __builtin_amdgcn_make_buffer_rsrc((void*)p.x0, 0, 2048 * 640 * 2, 0x00020000);
    const int pt = 4 * rb + (c16 >> 2), a = c16 & 3, u = lane >> 3;
    const unsigned xrow = (unsigned)(32 * pt + sigma(8 * a + (lane & 7))) * 1280u + (unsigned)(20 * w + u) * 16u;
    v4u x0a, x0b, x0c; v2u etag;
    asm volatile("buffer_load_dwordx4 %0, %4, %6, 0 offen\n\tbuffer_load_dwordx4 %1, %4, %6, 0 offen offset:128\n\tbuffer_load_dwordx4 %2, %5, %6, 0 offen\n\t"
                 "buffer_load_dwordx2 %3, off, %7, 0 sc1"
                 : "=&v"(x0a), "=&v"(x0b), "=&v"(x0c), "=&v"(etag) : "v"(xrow), "v"(u < 4 ? xrow + 256 : OOB), "s"(rX), "s"(rs) : "memory");
    wd_groups(0, 3, w, 5 * (c16 & 7), wdv, rWd);
    unsigned xdst = OFF_XA + (unsigned)(pt * 80 + 20 * w) * 512u + a * 128, xv = (unsigned)u * 512u + (lane & 7) * 16;
    unsigned sdst = OFF_STA + (unsigned)(2 * w * 2048 + pt * 32 + 8 * a) * 8u, fdst = fline(0, FL_X, pt * 2) + (a * 4 + w) * 8;
    PIECE_VARS();
    const rsrc_t rW = __builtin_amdgcn_make_buffer_rsrc((void*)(isw1 ? p.w1 : p.w2), 0, haspiece ? 640 * 1728 * 2 : 0, 0x00020000);
    unsigned pitch = isw1 ? 3456u : 1280u;
    unsigned pvo = (unsigned)(160 * kq + 40 * w + (lane >> 3)) * pitch + (unsigned)(128 * cblk) + (lane & 7) * 16;
    asm volatile("" : "+s"(xdst), "+s"(sdst), "+s"(fdst), "+s"(pitch));
    asm volatile("s_waitcnt vmcnt(%6)" : "+v"(x0a), "+v"(x0b), "+v"(x0c), "+v"(etag), "+v"(xv), "+v"(pvo) : "n"(15) : "memory");
    { const u64 e = (((u64)rfl(etag.y) << 32) | rfl(etag.x)) + 1ull; ep0 = (unsigned)e; ep1 = (unsigned)(e >> 32); }
    st128(x0a, rs, xv, xdst);
    st128(x0b, rs, xv + 4096, xdst);
    st128(x0c, rs, u < 4 ? xv + 8192 : OOB, xdst);
    f32x2 v[3][4];
#pragma unroll
    for (int e = 0; e < 4; ++e) { v[0][e] = f32x2{bflo(x0a[e]), bfhi(x0a[e])}; v[1][e] = f32x2{bflo(x0b[e]), bfhi(x0b[e])}; v[2][e] = f32x2{bflo(x0c[e]), bfhi(x0c[e])}; }
    float sm[3];
#pragma unroll
    for (int i = 0; i < 3; ++i) { const f32x2 s2 = (v[i][0] + v[i][1]) + (v[i][2] + v[i][3]); sm[i] = s2[0] + s2[1]; }
    const float m0 = sum_x32(sum_x16(sum_x8(u < 2 ? sm[0] + sm[1] : sm[0]))) * (1.0f / 80.0f), m1 = sum_x32(sum_x16(sum_x8(u < 2 ? sm[2] : sm[1] + sm[2]))) * (1.0f / 80.0f);
    const float mi[3] = {m0, u < 2 ? m0 : m1, m1};
#pragma unroll
    for (int i = 0; i < 3; ++i) {
      f32x2 q2 = {0.f, 0.f};
#pragma unroll
      for (int e = 0; e < 4; ++e) { const f32x2 d = v[i][e] - mi[i]; q2 += d * d; }
      sm[i] = q2[0] + q2[1];
    }
    if (u >= 4) sm[2] = 0.f;
    const float q0 = sum_x32(sum_x16(sum_x8(u < 2 ? sm[0] + sm[1] : sm[0]))), q1 = sum_x32(sum_x16(sum_x8(u < 2 ? sm[2] : sm[1] + sm[2])));
    __builtin_amdgcn_raw_buffer_store_b64(u == 0 ? v2u{__float_as_uint(m0), __float_as_uint(q0)} : v2u{__float_as_uint(m1), __float_as_uint(q1)}, rs,
                                          u < 2 ? (unsigned)(u * 2048 + (lane & 7)) * 8u : OOB, sdst, SC1);
    waitv<0>();
    __builtin_amdgcn_raw_buffer_store_b64(v2u{ep0, ep1}, rs, lane < 2 ? lane * 128u : OOB, fdst, SC1);
    wd_groups(3, 5, w, 5 * (c16 & 7), wdv, rWd);
#pragma unroll
    for (int i = 0; i < 5; ++i) dma(L_PT + w * 5120 + i * 1024, pvo, rW, i * 8 * pitch);
  }

#pragma unroll 1
  for (int ly = 0; ly < 2; ++ly) {
    {
      PHASE_VARS();
      const int pt = 4 * rb + w;
      const unsigned a1_sp = OFF_XA + ly * XU_B + pt * 40960, a1_ss = OFF_STA + ly * STA_B + pt * 256, a1_c0 = w * 2048;
      const unsigned a1_sg = half ? 0xbf800000u : 0x3f800000u;
      const unsigned ob = OFF_ABT + ly * ABT_B + pt * 2048;
#define A1_O(n) (ob + ((5 * c16 + (n)) >> 1) * 131072 + ((5 * c16 + (n)) & 1) * 256)
      const unsigned a1_o0 = A1_O(0), a1_o1 = A1_O(1), a1_o2 = A1_O(2), a1_o3 = A1_O(3), a1_o4 = A1_O(4);
      const unsigned a1_fo = A1_FO;
      const unsigned a1_vp = q * 512 + r * 16, a1_vs = (hh * 8192 + j32) * 8;
      const unsigned a1_l0 = r * 128 + (q ^ (r >> 1)) * 16, a1_l1 = a1_l0 ^ 64, a1_l2 = a1_l0 + 61440, a1_l3 = a1_l1 + 61440;
      const unsigned a1_ly = ly, a1_w0 = L_CSD + (w * 16 + r) * 4, a1_w1 = L_CSD + r * 4, a1_sw = L_SW + w * 256 + j32 * 8, a1_sr = L_SW + w * 256 + q * 32;
      const unsigned a1_v2 = lane < 2 ? lane * 128u : OOB;
      v4u a1_S0 = {(unsigned)(a1_sp), (unsigned)(a1_ss), (unsigned)(a1_c0), (unsigned)(a1_sg)}; v4u a1_S1 = {(unsigned)(a1_o0), (unsigned)(a1_o1), (unsigned)(a1_o2), (unsigned)(a1_o3)}; v4u a1_S2 = {(unsigned)(a1_o4), (unsigned)(a1_fo), (unsigned)(a1_ly), (unsigned)(a1_ly)}; v4u a1_V0 = {ep0, ep1, (unsigned)(a1_vp), (unsigned)(a1_vs)}; v4u a1_V1 = {(unsigned)(a1_l0), (unsigned)(a1_l1), (unsigned)(a1_l2), (unsigned)(a1_l3)}; v4u a1_V2 = {(unsigned)(a1_w0), (unsigned)(a1_w1), (unsigned)(a1_sw), (unsigned)(a1_sr)}; v4u a1_V3 = {(unsigned)(a1_v2), (unsigned)(a1_v2), (unsigned)(a1_v2), (unsigned)(a1_v2)};
      asm volatile("" : "+{s[36:39]}"(a1_S0), "+{s[40:43]}"(a1_S1), "+{s[44:47]}"(a1_S2));
      asm volatile("" : "+{v[32:35]}"(a1_V0), "+{v[36:39]}"(a1_V1), "+{v[40:43]}"(a1_V2), "+{v[44:47]}"(a1_V3));
      poll_spin((lane & 15) * 8, rs, fline(ly, FL_X, pt * 2 + half), v2u{ep0, ep1});
      asm volatile("s_add_u32 s48, s36, 0\n\t"
"buffer_load_dwordx4 v[124:127], v34, %[rs], s48 offen sc1\n\t"
"buffer_load_dwordx4 v[128:131], v34, %[rs], s48 offen offset:256 sc1\n\t"
"s_add_u32 s48, s36, 2048\n\t"
"buffer_load_dwordx4 v[132:135], v34, %[rs], s48 offen sc1\n\t"
"buffer_load_dwordx4 v[136:139], v34, %[rs], s48 offen offset:256 sc1\n\t"
"s_add_u32 s48, s37, 0\n\t"
"buffer_load_dwordx2 v[212:213], v35, %[rs], s48 offen sc1\n\t"
"s_add_u32 s48, s37, 16384\n\t"
"buffer_load_dwordx2 v[214:215], v35, %[rs], s48 offen sc1\n\t"
"s_add_u32 s48, s37, 32768\n\t"
"buffer_load_dwordx2 v[216:217], v35, %[rs], s48 offen sc1\n\t"
"s_add_u32 s48, s37, 49152\n\t"
"buffer_load_dwordx2 v[218:219], v35, %[rs], s48 offen sc1\n\t"
"s_add_u32 s48, s36, 4096\n\t"
"buffer_load_dwordx4 v[140:143], v34, %[rs], s48 offen sc1\n\t"
"buffer_load_dwordx4 v[144:147], v34, %[rs], s48 offen offset:256 sc1\n\t"
"s_add_u32 s48, s36, 6144\n\t"
"buffer_load_dwordx4 v[148:151], v34, %[rs], s48 offen sc1\n\t"
"buffer_load_dwordx4 v[152:155], v34, %[rs], s48 offen offset:256 sc1\n\t"
"s_add_u32 s48, s36, 8192\n\t"
"buffer_load_dwordx4 v[156:159], v34, %[rs], s48 offen sc1\n\t"
"buffer_load_dwordx4 v[160:163], v34, %[rs], s48 offen offset:256 sc1\n\t"
"s_add_u32 s48, s36, 10240\n\t"
"buffer_load_dwordx4 v[164:167], v34, %[rs], s48 offen sc1\n\t"
"buffer_load_dwordx4 v[168:171], v34, %[rs], s48 offen offset:256 sc1\n\t"
"s_waitcnt vmcnt(16) lgkmcnt(0)\n\t"
"s_barrier\n\t"
"s_cmp_lg_u32 s46, 0\n\t"
"s_cbranch_scc1 L_cs_skip_%=\n\t"
"v_mov_b32 v220, 0x3f803f80\n\t"
"v_mov_b32 v221, 0x3f803f80\n\t"
"v_mov_b32 v222, 0x3f803f80\n\t"
"v_mov_b32 v223, 0x3f803f80\n\t"
"v_mov_b32 v64, 0\n\t"
"v_mov_b32 v65, 0\n\t"
"v_mov_b32 v66, 0\n\t"
"v_mov_b32 v67, 0\n\t"
"v_mov_b32 v68, 0\n\t"
"v_mov_b32 v69, 0\n\t"
"v_mov_b32 v70, 0\n\t"
"v_mov_b32 v71, 0\n\t"
"v_add_u32 v224, s38, v36\n\t"
"v_add_u32 v225, 8192, v36\n\t"
"v_add_u32 v226, s38, v37\n\t"
"v_add_u32 v227, 8192, v37\n\t"
"v_add_u32 v228, s38, v38\n\t"
"v_add_u32 v229, 8192, v38\n\t"
"v_add_u32 v230, s38, v39\n\t"
"v_add_u32 v231, 8192, v39\n\t"
"ds_read_b128 v[172:175], v224 offset:0\n\t"
"ds_read_b128 v[176:179], v225 offset:0\n\t"
"ds_read_b128 v[180:183], v226 offset:0\n\t"
"ds_read_b128 v[184:187], v227 offset:0\n\t"
"ds_read_b128 v[188:191], v224 offset:10240\n\t"
"ds_read_b128 v[192:195], v225 offset:10240\n\t"
"ds_read_b128 v[196:199], v226 offset:10240\n\t"
"ds_read_b128 v[200:203], v227 offset:10240\n\t"
"s_waitcnt lgkmcnt(6)\n\t"
"v_mfma_f32_16x16x32_bf16 v[64:67], v[220:223], v[172:175], v[64:67]\n\t"
"ds_read_b128 v[172:175], v224 offset:20480\n\t"
"v_mfma_f32_16x16x32_bf16 v[68:71], v[220:223], v[176:179], v[68:71]\n\t"
"ds_read_b128 v[176:179], v225 offset:20480\n\t"
"s_waitcnt lgkmcnt(6)\n\t"
"v_mfma_f32_16x16x32_bf16 v[64:67], v[220:223], v[180:183], v[64:67]\n\t"
"ds_read_b128 v[180:183], v226 offset:20480\n\t"
"v_mfma_f32_16x16x32_bf16 v[68:71], v[220:223], v[184:187], v[68:71]\n\t"
"ds_read_b128 v[184:187], v227 offset:20480\n\t"
"s_waitcnt lgkmcnt(6)\n\t"
"v_mfma_f32_16x16x32_bf16 v[64:67], v[220:223], v[188:191], v[64:67]\n\t"
"ds_read_b128 v[188:191], v224 offset:30720\n\t"
"v_mfma_f32_16x16x32_bf16 v[68:71], v[220:223], v[192:195], v[68:71]\n\t"
"ds_read_b128 v[192:195], v225 offset:30720\n\t"
"s_waitcnt lgkmcnt(6)\n\t"
"v_mfma_f32_16x16x32_bf16 v[64:67], v[220:223], v[196:199], v[64:67]\n\t"
"ds_read_b128 v[196:199], v226 offset:30720\n\t"
"v_mfma_f32_16x16x32_bf16 v[68:71], v[220:223], v[200:203], v[68:71]\n\t"
"ds_read_b128 v[200:203], v227 offset:30720\n\t"
"s_waitcnt lgkmcnt(6)\n\t"
"v_mfma_f32_16x16x32_bf16 v[64:67], v[220:223], v[172:175], v[64:67]\n\t"
"ds_read_b128 v[172:175], v224 offset:40960\n\t"
"v_mfma_f32_16x16x32_bf16 v[68:71], v[220:223], v[176:179], v[68:71]\n\t"
"ds_read_b128 v[176:179], v225 offset:40960\n\t"
"s_waitcnt lgkmcnt(6)\n\t"
"v_mfma_f32_16x16x32_bf16 v[64:67], v[220:223], v[180:183], v[64:67]\n\t"
"ds_read_b128 v[180:183], v226 offset:40960\n\t"
"v_mfma_f32_16x16x32_bf16 v[68:71], v[220:223], v[184:187], v[68:71]\n\t"
"ds_read_b128 v[184:187], v227 offset:40960\n\t"
"s_waitcnt lgkmcnt(6)\n\t"
"v_mfma_f32_16x16x32_bf16 v[64:67], v[220:223], v[188:191], v[64:67]\n\t"
"ds_read_b128 v[188:191], v224 offset:51200\n\t"
"v_mfma_f32_16x16x32_bf16 v[68:71], v[220:223], v[192:195], v[68:71]\n\t"
"ds_read_b128 v[192:195], v225 offset:51200\n\t"
"s_waitcnt lgkmcnt(6)\n\t"
"v_mfma_f32_16x16x32_bf16 v[64:67], v[220:223], v[196:199], v[64:67]\n\t"
"ds_read_b128 v[196:199], v226 offset:51200\n\t"
"v_mfma_f32_16x16x32_bf16 v[68:71], v[220:223], v[200:203], v[68:71]\n\t"
"ds_read_b128 v[200:203], v227 offset:51200\n\t"
"s_waitcnt lgkmcnt(6)\n\t"
"v_mfma_f32_16x16x32_bf16 v[64:67], v[220:223], v[172:175], v[64:67]\n\t"
"ds_read_b128 v[172:175], v228 offset:0\n\t"
"v_mfma_f32_16x16x32_bf16 v[68:71], v[220:223], v[176:179], v[68:71]\n\t"
"ds_read_b128 v[176:179], v229 offset:0\n\t"
"s_waitcnt lgkmcnt(6)\n\t"
"v_mfma_f32_16x16x32_bf16 v[64:67], v[220:223], v[180:183], v[64:67]\n\t"
"ds_read_b128 v[180:183], v230 offset:0\n\t"
"v_mfma_f32_16x16x32_bf16 v[68:71], v[220:223], v[184:187], v[68:71]\n\t"
"ds_read_b128 v[184:187], v231 offset:0\n\t"
"s_waitcnt lgkmcnt(6)\n\t"
"v_mfma_f32_16x16x32_bf16 v[64:67], v[220:223], v[188:191], v[64:67]\n\t"
"ds_read_b128 v[188:191], v228 offset:10240\n\t"
"v_mfma_f32_16x16x32_bf16 v[68:71], v[220:223], v[192:195], v[68:71]\n\t"
"ds_read_b128 v[192:195], v229 offset:10240\n\t"
"s_waitcnt lgkmcnt(6)\n\t"
"v_mfma_f32_16x16x32_bf16 v[64:67], v[220:223], v[196:199], v[64:67]\n\t"
"ds_read_b128 v[196:199], v230 offset:10240\n\t"
"v_mfma_f32_16x16x32_bf16 v[68:71], v[220:223], v[200:203], v[68:71]\n\t"
"ds_read_b128 v[200:203], v231 offset:10240\n\t"
"s_waitcnt lgkmcnt(6)\n\t"
"v_mfma_f32_16x16x32_bf16 v[64:67], v[220:223], v[172:175], v[64:67]\n\t"
"ds_read_b128 v[172:175], v228 offset:20480\n\t"
"v_mfma_f32_16x16x32_bf16 v[68:71], v[220:223], v[176:179], v[68:71]\n\t"
"ds_read_b128 v[176:179], v229 offset:20480\n\t"
"s_waitcnt lgkmcnt(6)\n\t"
"v_mfma_f32_16x16x32_bf16 v[64:67], v[220:223], v[180:183], v[64:67]\n\t"
"ds_read_b128 v[180:183], v230 offset:20480\n\t"
"v_mfma_f32_16x16x32_bf16 v[68:71], v[220:223], v[184:187], v[68:71]\n\t"
"ds_read_b128 v[184:187], v231 offset:20480\n\t"
"s_waitcnt lgkmcnt(6)\n\t"
"v_mfma_f32_16x16x32_bf16 v[64:67], v[220:223], v[188:191], v[64:67]\n\t"
"ds_read_b128 v[188:191], v228 offset:30720\n\t"
"v_mfma_f32_16x16x32_bf16 v[68:71], v[220:223], v[192:195], v[68:71]\n\t"
"ds_read_b128 v[192:195], v229 offset:30720\n\t"
"s_waitcnt lgkmcnt(6)\n\t"
"v_mfma_f32_16x16x32_bf16 v[64:67], v[220:223], v[196:199], v[64:67]\n\t"
"ds_read_b128 v[196:199], v230 offset:30720\n\t"
"v_mfma_f32_16x16x32_bf16 v[68:71], v[220:223], v[200:203], v[68:71]\n\t"
"ds_read_b128 v[200:203], v231 offset:30720\n\t"
"s_waitcnt lgkmcnt(6)\n\t"
"v_mfma_f32_16x16x32_bf16 v[64:67], v[220:223], v[172:175], v[64:67]\n\t"
"v_mfma_f32_16x16x32_bf16 v[68:71], v[220:223], v[176:179], v[68:71]\n\t"
"s_waitcnt lgkmcnt(4)\n\t"
"v_mfma_f32_16x16x32_bf16 v[64:67], v[220:223], v[180:183], v[64:67]\n\t"
"v_mfma_f32_16x16x32_bf16 v[68:71], v[220:223], v[184:187], v[68:71]\n\t"
"s_waitcnt lgkmcnt(2)\n\t"
"v_mfma_f32_16x16x32_bf16 v[64:67], v[220:223], v[188:191], v[64:67]\n\t"
"v_mfma_f32_16x16x32_bf16 v[68:71], v[220:223], v[192:195], v[68:71]\n\t"
"s_waitcnt lgkmcnt(0)\n\t"
"v_mfma_f32_16x16x32_bf16 v[64:67], v[220:223], v[196:199], v[64:67]\n\t"
"v_mfma_f32_16x16x32_bf16 v[68:71], v[220:223], v[200:203], v[68:71]\n\t"
"s_nop 7\n\t"
"s_nop 1\n\t"
"ds_write_b32 v40, v64\n\t"
"ds_write_b32 v41, v68 offset:256\n\t"
"L_cs_skip_%=:\n\t"
"ds_read_b128 v[104:107], v36 offset:0\n\t"
"ds_read_b128 v[108:111], v36 offset:2048\n\t"
"ds_read_b128 v[112:115], v36 offset:4096\n\t"
"ds_read_b128 v[116:119], v36 offset:6144\n\t"
"ds_read_b128 v[120:123], v36 offset:8192\n\t"
"v_mov_b32 v64, 0\n\t"
"v_mov_b32 v65, 0\n\t"
"v_mov_b32 v66, 0\n\t"
"v_mov_b32 v67, 0\n\t"
"v_mov_b32 v68, 0\n\t"
"v_mov_b32 v69, 0\n\t"
"v_mov_b32 v70, 0\n\t"
"v_mov_b32 v71, 0\n\t"
"v_mov_b32 v72, 0\n\t"
"v_mov_b32 v73, 0\n\t"
"v_mov_b32 v74, 0\n\t"
"v_mov_b32 v75, 0\n\t"
"v_mov_b32 v76, 0\n\t"
"v_mov_b32 v77, 0\n\t"
"v_mov_b32 v78, 0\n\t"
"v_mov_b32 v79, 0\n\t"
"v_mov_b32 v80, 0\n\t"
"v_mov_b32 v81, 0\n\t"
"v_mov_b32 v82, 0\n\t"
"v_mov_b32 v83, 0\n\t"
"v_mov_b32 v84, 0\n\t"
"v_mov_b32 v85, 0\n\t"
"v_mov_b32 v86, 0\n\t"
"v_mov_b32 v87, 0\n\t"
"v_mov_b32 v88, 0\n\t"
"v_mov_b32 v89, 0\n\t"
"v_mov_b32 v90, 0\n\t"
"v_mov_b32 v91, 0\n\t"
"v_mov_b32 v92, 0\n\t"
"v_mov_b32 v93, 0\n\t"
"v_mov_b32 v94, 0\n\t"
"v_mov_b32 v95, 0\n\t"
"v_mov_b32 v96, 0\n\t"
"v_mov_b32 v97, 0\n\t"
"v_mov_b32 v98, 0\n\t"
"v_mov_b32 v99, 0\n\t"
"v_mov_b32 v100, 0\n\t"
"v_mov_b32 v101, 0\n\t"
"v_mov_b32 v102, 0\n\t"
"v_mov_b32 v103, 0\n\t"
"s_waitcnt vmcnt(14) lgkmcnt(3)\n\t"
"v_mfma_f32_16x16x32_bf16 v[64:67], v[124:127], v[104:107], v[64:67]\n\t"
"s_add_u32 s48, s36, 12288\n\t"
"v_mfma_f32_16x16x32_bf16 v[68:71], v[128:131], v[104:107], v[68:71]\n\t"
"ds_read_b128 v[104:107], v37 offset:0\n\t"
"v_mfma_f32_16x16x32_bf16 v[72:75], v[124:127], v[108:111], v[72:75]\n\t"
"buffer_load_dwordx4 v[172:175], v34, %[rs], s48 offen sc1\n\t"
"v_mfma_f32_16x16x32_bf16 v[76:79], v[128:131], v[108:111], v[76:79]\n\t"
"ds_read_b128 v[108:111], v37 offset:2048\n\t"
"s_waitcnt lgkmcnt(3)\n\t"
"v_mfma_f32_16x16x32_bf16 v[80:83], v[124:127], v[112:115], v[80:83]\n\t"
"buffer_load_dwordx4 v[176:179], v34, %[rs], s48 offen offset:256 sc1\n\t"
"v_mfma_f32_16x16x32_bf16 v[84:87], v[128:131], v[112:115], v[84:87]\n\t"
"ds_read_b128 v[112:115], v37 offset:4096\n\t"
"s_add_u32 s48, s36, 14336\n\t"
"v_mfma_f32_16x16x32_bf16 v[88:91], v[124:127], v[116:119], v[88:91]\n\t"
"buffer_load_dwordx4 v[180:183], v34, %[rs], s48 offen sc1\n\t"
"v_mfma_f32_16x16x32_bf16 v[92:95], v[128:131], v[116:119], v[92:95]\n\t"
"ds_read_b128 v[116:119], v37 offset:6144\n\t"
"s_waitcnt lgkmcnt(3)\n\t"
"v_mfma_f32_16x16x32_bf16 v[96:99], v[124:127], v[120:123], v[96:99]\n\t"
"buffer_load_dwordx4 v[184:187], v34, %[rs], s48 offen offset:256 sc1\n\t"
"v_mfma_f32_16x16x32_bf16 v[100:103], v[128:131], v[120:123], v[100:103]\n\t"
"ds_read_b128 v[120:123], v37 offset:8192\n\t"
"s_waitcnt vmcnt(16)\n\t"
"v_mfma_f32_16x16x32_bf16 v[64:67], v[132:135], v[104:107], v[64:67]\n\t"
"s_add_u32 s48, s36, 16384\n\t"
"v_mfma_f32_16x16x32_bf16 v[68:71], v[136:139], v[104:107], v[68:71]\n\t"
"ds_read_b128 v[104:107], v36 offset:10240\n\t"
"s_waitcnt lgkmcnt(3)\n\t"
"v_mfma_f32_16x16x32_bf16 v[72:75], v[132:135], v[108:111], v[72:75]\n\t"
"buffer_load_dwordx4 v[188:191], v34, %[rs], s48 offen sc1\n\t"
"v_mfma_f32_16x16x32_bf16 v[76:79], v[136:139], v[108:111], v[76:79]\n\t"
"ds_read_b128 v[108:111], v36 offset:12288\n\t"
"v_mfma_f32_16x16x32_bf16 v[80:83], v[132:135], v[112:115], v[80:83]\n\t"
"buffer_load_dwordx4 v[192:195], v34, %[rs], s48 offen offset:256 sc1\n\t"
"v_mfma_f32_16x16x32_bf16 v[84:87], v[136:139], v[112:115], v[84:87]\n\t"
"ds_read_b128 v[112:115], v36 offset:14336\n\t"
"s_waitcnt lgkmcnt(3)\n\t"
"v_mfma_f32_16x16x32_bf16 v[88:91], v[132:135], v[116:119], v[88:91]\n\t"
"s_add_u32 s48, s36, 18432\n\t"
"v_mfma_f32_16x16x32_bf16 v[92:95], v[136:139], v[116:119], v[92:95]\n\t"
"ds_read_b128 v[116:119], v36 offset:16384\n\t"
"v_mfma_f32_16x16x32_bf16 v[96:99], v[132:135], v[120:123], v[96:99]\n\t"
"buffer_load_dwordx4 v[196:199], v34, %[rs], s48 offen sc1\n\t"
"v_mfma_f32_16x16x32_bf16 v[100:103], v[136:139], v[120:123], v[100:103]\n\t"
"ds_read_b128 v[120:123], v36 offset:18432\n\t"
"s_waitcnt vmcnt(13) lgkmcnt(3)\n\t"
"v_mfma_f32_16x16x32_bf16 v[64:67], v[140:143], v[104:107], v[64:67]\n\t"
"buffer_load_dwordx4 v[200:203], v34, %[rs], s48 offen offset:256 sc1\n\t"
"v_mfma_f32_16x16x32_bf16 v[68:71], v[144:147], v[104:107], v[68:71]\n\t"
"ds_read_b128 v[104:107], v37 offset:10240\n\t"
"s_add_u32 s48, s36, 20480\n\t"
"v_mfma_f32_16x16x32_bf16 v[72:75], v[140:143], v[108:111], v[72:75]\n\t"
"buffer_load_dwordx4 v[204:207], v34, %[rs], s48 offen sc1\n\t"
"v_mfma_f32_16x16x32_bf16 v[76:79], v[144:147], v[108:111], v[76:79]\n\t"
"ds_read_b128 v[108:111], v37 offset:12288\n\t"
"s_waitcnt lgkmcnt(3)\n\t"
"v_mfma_f32_16x16x32_bf16 v[80:83], v[140:143], v[112:115], v[80:83]\n\t"
"s_barrier\n\t"
"v_mfma_f32_16x16x32_bf16 v[84:87], v[144:147], v[112:115], v[84:87]\n\t"
"ds_read_b128 v[112:115], v37 offset:14336\n\t"
"v_mfma_f32_16x16x32_bf16 v[88:91], v[140:143], v[116:119], v[88:91]\n\t"
"buffer_load_dwordx4 v[208:211], v34, %[rs], s48 offen offset:256 sc1\n\t"
"v_mfma_f32_16x16x32_bf16 v[92:95], v[144:147], v[116:119], v[92:95]\n\t"
"ds_read_b128 v[116:119], v37 offset:16384\n\t"
"s_waitcnt lgkmcnt(3)\n\t"
"v_mfma_f32_16x16x32_bf16 v[96:99], v[140:143], v[120:123], v[96:99]\n\t"
"s_add_u32 s48, s36, 22528\n\t"
"v_mfma_f32_16x16x32_bf16 v[100:103], v[144:147], v[120:123], v[100:103]\n\t"
"ds_read_b128 v[120:123], v37 offset:18432\n\t"
"s_waitcnt vmcnt(14)\n\t"
"v_mfma_f32_16x16x32_bf16 v[64:67], v[148:151], v[104:107], v[64:67]\n\t"
"buffer_load_dwordx4 v[124:127], v34, %[rs], s48 offen sc1\n\t"
"v_mfma_f32_16x16x32_bf16 v[68:71], v[152:155], v[104:107], v[68:71]\n\t"
"ds_read_b128 v[104:107], v36 offset:20480\n\t"
"s_waitcnt lgkmcnt(3)\n\t"
"v_mfma_f32_16x16x32_bf16 v[72:75], v[148:151], v[108:111], v[72:75]\n\t"
"buffer_load_dwordx4 v[128:131], v34, %[rs], s48 offen offset:256 sc1\n\t"
"v_mfma_f32_16x16x32_bf16 v[76:79], v[152:155], v[108:111], v[76:79]\n\t"
"ds_read_b128 v[108:111], v36 offset:22528\n\t"
"s_add_u32 s48, s36, 24576\n\t"
"v_mfma_f32_16x16x32_bf16 v[80:83], v[148:151], v[112:115], v[80:83]\n\t"
"buffer_load_dwordx4 v[132:135], v34, %[rs], s48 offen sc1\n\t"
"v_mfma_f32_16x16x32_bf16 v[84:87], v[152:155], v[112:115], v[84:87]\n\t"
"ds_read_b128 v[112:115], v36 offset:24576\n\t"
"s_waitcnt lgkmcnt(3)\n\t"
"v_mfma_f32_16x16x32_bf16 v[88:91], v[148:151], v[116:119], v[88:91]\n\t"
"buffer_load_dwordx4 v[136:139], v34, %[rs], s48 offen offset:256 sc1\n\t"
"v_mfma_f32_16x16x32_bf16 v[92:95], v[152:155], v[116:119], v[92:95]\n\t"
"ds_read_b128 v[116:119], v36 offset:26624\n\t"
"s_add_u32 s48, s36, 26624\n\t"
"v_mfma_f32_16x16x32_bf16 v[96:99], v[148:151], v[120:123], v[96:99]\n\t"
"buffer_load_dwordx4 v[140:143], v34, %[rs], s48 offen sc1\n\t"
"v_mfma_f32_16x16x32_bf16 v[100:103], v[152:155], v[120:123], v[100:103]\n\t"
"ds_read_b128 v[120:123], v36 offset:28672\n\t"
"s_waitcnt vmcnt(17) lgkmcnt(3)\n\t"
"v_mfma_f32_16x16x32_bf16 v[64:67], v[156:159], v[104:107], v[64:67]\n\t"
"buffer_load_dwordx4 v[144:147], v34, %[rs], s48 offen offset:256 sc1\n\t"
"v_mfma_f32_16x16x32_bf16 v[68:71], v[160:163], v[104:107], v[68:71]\n\t"
"ds_read_b128 v[104:107], v37 offset:20480\n\t"
"s_add_u32 s48, s36, 28672\n\t"
"v_mfma_f32_16x16x32_bf16 v[72:75], v[156:159], v[108:111], v[72:75]\n\t"
"buffer_load_dwordx4 v[148:151], v34, %[rs], s48 offen sc1\n\t"
"v_mfma_f32_16x16x32_bf16 v[76:79], v[160:163], v[108:111], v[76:79]\n\t"
"ds_read_b128 v[108:111], v37 offset:22528\n\t"
"s_waitcnt lgkmcnt(3)\n\t"
"v_mfma_f32_16x16x32_bf16 v[80:83], v[156:159], v[112:115], v[80:83]\n\t"
"v_add_f32 v242, v212, v214\n\t"
"v_add_f32 v243, v216, v218\n\t"
"v_mfma_f32_16x16x32_bf16 v[84:87], v[160:163], v[112:115], v[84:87]\n\t"
"ds_read_b128 v[112:115], v37 offset:24576\n\t"
"v_add_f32 v242, v242, v243\n\t"
"v_mfma_f32_16x16x32_bf16 v[88:91], v[156:159], v[116:119], v[88:91]\n\t"
"buffer_load_dwordx4 v[152:155], v34, %[rs], s48 offen offset:256 sc1\n\t"
"v_mfma_f32_16x16x32_bf16 v[92:95], v[160:163], v[116:119], v[92:95]\n\t"
"ds_read_b128 v[116:119], v37 offset:26624\n\t"
"s_waitcnt lgkmcnt(3)\n\t"
"v_mfma_f32_16x16x32_bf16 v[96:99], v[156:159], v[120:123], v[96:99]\n\t"
"v_mov_b32 v243, v242\n\t"
"s_nop 1\n\t"
"v_mfma_f32_16x16x32_bf16 v[100:103], v[160:163], v[120:123], v[100:103]\n\t"
"ds_read_b128 v[120:123], v37 offset:28672\n\t"
"s_waitcnt vmcnt(18)\n\t"
"v_mfma_f32_16x16x32_bf16 v[64:67], v[164:167], v[104:107], v[64:67]\n\t"
"s_add_u32 s48, s36, 30720\n\t"
"v_mfma_f32_16x16x32_bf16 v[68:71], v[168:171], v[104:107], v[68:71]\n\t"
"ds_read_b128 v[104:107], v36 offset:30720\n\t"
"s_waitcnt lgkmcnt(3)\n\t"
"v_mfma_f32_16x16x32_bf16 v[72:75], v[164:167], v[108:111], v[72:75]\n\t"
"buffer_load_dwordx4 v[156:159], v34, %[rs], s48 offen sc1\n\t"
"v_mfma_f32_16x16x32_bf16 v[76:79], v[168:171], v[108:111], v[76:79]\n\t"
"ds_read_b128 v[108:111], v36 offset:32768\n\t"
"v_mfma_f32_16x16x32_bf16 v[80:83], v[164:167], v[112:115], v[80:83]\n\t"
"v_permlane32_swap_b32 v242, v243\n\t"
"v_add_f32 v242, v242, v243\n\t"
"v_mfma_f32_16x16x32_bf16 v[84:87], v[168:171], v[112:115], v[84:87]\n\t"
"ds_read_b128 v[112:115], v36 offset:34816\n\t"
"s_waitcnt lgkmcnt(3)\n\t"
"v_mfma_f32_16x16x32_bf16 v[88:91], v[164:167], v[116:119], v[88:91]\n\t"
"buffer_load_dwordx4 v[160:163], v34, %[rs], s48 offen offset:256 sc1\n\t"
"v_mfma_f32_16x16x32_bf16 v[92:95], v[168:171], v[116:119], v[92:95]\n\t"
"ds_read_b128 v[116:119], v36 offset:36864\n\t"
"v_mul_f32 v242, 0x3e000000, v242\n\t"
"v_mfma_f32_16x16x32_bf16 v[96:99], v[164:167], v[120:123], v[96:99]\n\t"
"v_sub_f32 v212, v212, v242\n\t"
"v_sub_f32 v214, v214, v242\n\t"
"v_mfma_f32_16x16x32_bf16 v[100:103], v[168:171], v[120:123], v[100:103]\n\t"
"ds_read_b128 v[120:123], v36 offset:38912\n\t"
"s_waitcnt vmcnt(18) lgkmcnt(3)\n\t"
"v_mfma_f32_16x16x32_bf16 v[64:67], v[172:175], v[104:107], v[64:67]\n\t"
"s_add_u32 s48, s36, 32768\n\t"
"v_sub_f32 v216, v216, v242\n\t"
"v_mfma_f32_16x16x32_bf16 v[68:71], v[176:179], v[104:107], v[68:71]\n\t"
"ds_read_b128 v[104:107], v37 offset:30720\n\t"
"v_sub_f32 v218, v218, v242\n\t"
"v_mfma_f32_16x16x32_bf16 v[72:75], v[172:175], v[108:111], v[72:75]\n\t"
"buffer_load_dwordx4 v[164:167], v34, %[rs], s48 offen sc1\n\t"
"v_mfma_f32_16x16x32_bf16 v[76:79], v[176:179], v[108:111], v[76:79]\n\t"
"ds_read_b128 v[108:111], v37 offset:32768\n\t"
"s_waitcnt lgkmcnt(3)\n\t"
"v_mfma_f32_16x16x32_bf16 v[80:83], v[172:175], v[112:115], v[80:83]\n\t"
"v_mul_f32 v244, v212, v212\n\t"
"v_fmac_f32 v244, v214, v214\n\t"
"v_mfma_f32_16x16x32_bf16 v[84:87], v[176:179], v[112:115], v[84:87]\n\t"
"ds_read_b128 v[112:115], v37 offset:34816\n\t"
"v_fmac_f32 v244, v216, v216\n\t"
"v_mfma_f32_16x16x32_bf16 v[88:91], v[172:175], v[116:119], v[88:91]\n\t"
"buffer_load_dwordx4 v[168:171], v34, %[rs], s48 offen offset:256 sc1\n\t"
"v_mfma_f32_16x16x32_bf16 v[92:95], v[176:179], v[116:119], v[92:95]\n\t"
"ds_read_b128 v[116:119], v37 offset:36864\n\t"
"s_waitcnt lgkmcnt(3)\n\t"
"v_mfma_f32_16x16x32_bf16 v[96:99], v[172:175], v[120:123], v[96:99]\n\t"
"v_fmac_f32 v244, v218, v218\n\t"
"v_add_f32 v213, v213, v215\n\t"
"v_mfma_f32_16x16x32_bf16 v[100:103], v[176:179], v[120:123], v[100:103]\n\t"
"ds_read_b128 v[120:123], v37 offset:38912\n\t"
"s_waitcnt vmcnt(18)\n\t"
"v_mfma_f32_16x16x32_bf16 v[64:67], v[180:183], v[104:107], v[64:67]\n\t"
"s_add_u32 s48, s36, 34816\n\t"
"v_add_f32 v217, v217, v219\n\t"
"v_mfma_f32_16x16x32_bf16 v[68:71], v[184:187], v[104:107], v[68:71]\n\t"
"ds_read_b128 v[104:107], v36 offset:40960\n\t"
"s_waitcnt lgkmcnt(3)\n\t"
"v_mfma_f32_16x16x32_bf16 v[72:75], v[180:183], v[108:111], v[72:75]\n\t"
"buffer_load_dwordx4 v[172:175], v34, %[rs], s48 offen sc1\n\t"
"v_mfma_f32_16x16x32_bf16 v[76:79], v[184:187], v[108:111], v[76:79]\n\t"
"ds_read_b128 v[108:111], v36 offset:43008\n\t"
"v_add_f32 v213, v213, v217\n\t"
"v_mfma_f32_16x16x32_bf16 v[80:83], v[180:183], v[112:115], v[80:83]\n\t"
"v_fmac_f32 v213, 0x42a00000, v244\n\t"
"v_mfma_f32_16x16x32_bf16 v[84:87], v[184:187], v[112:115], v[84:87]\n\t"
"ds_read_b128 v[112:115], v36 offset:45056\n\t"
"s_waitcnt lgkmcnt(3)\n\t"
"v_mfma_f32_16x16x32_bf16 v[88:91], v[180:183], v[116:119], v[88:91]\n\t"
"buffer_load_dwordx4 v[176:179], v34, %[rs], s48 offen offset:256 sc1\n\t"
"v_mfma_f32_16x16x32_bf16 v[92:95], v[184:187], v[116:119], v[92:95]\n\t"
"ds_read_b128 v[116:119], v36 offset:47104\n\t"
"v_mfma_f32_16x16x32_bf16 v[96:99], v[180:183], v[120:123], v[96:99]\n\t"
"v_mov_b32 v243, v213\n\t"
"s_nop 1\n\t"
"v_mfma_f32_16x16x32_bf16 v[100:103], v[184:187], v[120:123], v[100:103]\n\t"
"ds_read_b128 v[120:123], v36 offset:49152\n\t"
"s_waitcnt vmcnt(18) lgkmcnt(3)\n\t"
"v_mfma_f32_16x16x32_bf16 v[64:67], v[188:191], v[104:107], v[64:67]\n\t"
"s_add_u32 s48, s36, 36864\n\t"
"v_mfma_f32_16x16x32_bf16 v[68:71], v[192:195], v[104:107], v[68:71]\n\t"
"ds_read_b128 v[104:107], v37 offset:40960\n\t"
"v_mfma_f32_16x16x32_bf16 v[72:75], v[188:191], v[108:111], v[72:75]\n\t"
"buffer_load_dwordx4 v[180:183], v34, %[rs], s48 offen sc1\n\t"
"v_mfma_f32_16x16x32_bf16 v[76:79], v[192:195], v[108:111], v[76:79]\n\t"
"ds_read_b128 v[108:111], v37 offset:43008\n\t"
"s_waitcnt lgkmcnt(3)\n\t"
"v_mfma_f32_16x16x32_bf16 v[80:83], v[188:191], v[112:115], v[80:83]\n\t"
"v_permlane32_swap_b32 v213, v243\n\t"
"v_add_f32 v213, v213, v243\n\t"
"v_mfma_f32_16x16x32_bf16 v[84:87], v[192:195], v[112:115], v[84:87]\n\t"
"ds_read_b128 v[112:115], v37 offset:45056\n\t"
"v_mfma_f32_16x16x32_bf16 v[88:91], v[188:191], v[116:119], v[88:91]\n\t"
"buffer_load_dwordx4 v[184:187], v34, %[rs], s48 offen offset:256 sc1\n\t"
"v_mfma_f32_16x16x32_bf16 v[92:95], v[192:195], v[116:119], v[92:95]\n\t"
"ds_read_b128 v[116:119], v37 offset:47104\n\t"
"s_waitcnt lgkmcnt(3)\n\t"
"v_mfma_f32_16x16x32_bf16 v[96:99], v[188:191], v[120:123], v[96:99]\n\t"
"v_mov_b32 v243, 0x3727c5ac\n\t"
"v_fmac_f32 v243, 0x3acccccd, v213\n\t"
"v_mfma_f32_16x16x32_bf16 v[100:103], v[192:195], v[120:123], v[100:103]\n\t"
"ds_read_b128 v[120:123], v37 offset:49152\n\t"
"s_waitcnt vmcnt(18)\n\t"
"v_mfma_f32_16x16x32_bf16 v[64:67], v[196:199], v[104:107], v[64:67]\n\t"
"s_add_u32 s48, s36, 38912\n\t"
"v_rsq_f32 v243, v243\n\t"
"v_mfma_f32_16x16x32_bf16 v[68:71], v[200:203], v[104:107], v[68:71]\n\t"
"ds_read_b128 v[104:107], v36 offset:51200\n\t"
"s_waitcnt lgkmcnt(3)\n\t"
"v_mfma_f32_16x16x32_bf16 v[72:75], v[196:199], v[108:111], v[72:75]\n\t"
"buffer_load_dwordx4 v[188:191], v34, %[rs], s48 offen sc1\n\t"
"v_mfma_f32_16x16x32_bf16 v[76:79], v[200:203], v[108:111], v[76:79]\n\t"
"ds_read_b128 v[108:111], v36 offset:53248\n\t"
"v_mfma_f32_16x16x32_bf16 v[80:83], v[196:199], v[112:115], v[80:83]\n\t"
"s_nop 0\n\t"
"v_mul_f32 v243, s39, v243\n\t"
"v_mfma_f32_16x16x32_bf16 v[84:87], v[200:203], v[112:115], v[84:87]\n\t"
"ds_read_b128 v[112:115], v36 offset:55296\n\t"
"s_waitcnt lgkmcnt(3)\n\t"
"v_mfma_f32_16x16x32_bf16 v[88:91], v[196:199], v[116:119], v[88:91]\n\t"
"buffer_load_dwordx4 v[192:195], v34, %[rs], s48 offen offset:256 sc1\n\t"
"v_mfma_f32_16x16x32_bf16 v[92:95], v[200:203], v[116:119], v[92:95]\n\t"
"ds_read_b128 v[116:119], v36 offset:57344\n\t"
"v_mfma_f32_16x16x32_bf16 v[96:99], v[196:199], v[120:123], v[96:99]\n\t"
"ds_write_b64 v42, v[242:243]\n\t"
"ds_read_b128 v[220:223], v43 offset:0\n\t"
"ds_read_b128 v[224:227], v43 offset:16\n\t"
"v_mfma_f32_16x16x32_bf16 v[100:103], v[200:203], v[120:123], v[100:103]\n\t"
"ds_read_b128 v[120:123], v36 offset:59392\n\t"
"s_waitcnt vmcnt(18) lgkmcnt(6)\n\t"
"v_mfma_f32_16x16x32_bf16 v[64:67], v[204:207], v[104:107], v[64:67]\n\t"
"ds_read_b128 v[228:231], v43 offset:128\n\t"
"ds_read_b128 v[232:235], v43 offset:144\n\t"
"ds_read_b32 v236, v41 offset:0\n\t"
"v_mfma_f32_16x16x32_bf16 v[68:71], v[208:211], v[104:107], v[68:71]\n\t"
"ds_read_b128 v[104:107], v37 offset:51200\n\t"
"v_mfma_f32_16x16x32_bf16 v[72:75], v[204:207], v[108:111], v[72:75]\n\t"
"ds_read_b32 v237, v41 offset:64\n\t"
"ds_read_b32 v238, v41 offset:128\n\t"
"ds_read_b32 v239, v41 offset:192\n\t"
"v_mfma_f32_16x16x32_bf16 v[76:79], v[208:211], v[108:111], v[76:79]\n\t"
"ds_read_b128 v[108:111], v37 offset:53248\n\t"
"s_waitcnt lgkmcnt(12)\n\t"
"v_mfma_f32_16x16x32_bf16 v[80:83], v[204:207], v[112:115], v[80:83]\n\t"
"ds_read_b32 v240, v41 offset:256\n\t"
"v_mfma_f32_16x16x32_bf16 v[84:87], v[208:211], v[112:115], v[84:87]\n\t"
"ds_read_b128 v[112:115], v37 offset:55296\n\t"
"v_mfma_f32_16x16x32_bf16 v[88:91], v[204:207], v[116:119], v[88:91]\n\t"
"s_waitcnt lgkmcnt(1)\n\t"
"v_fma_f32 v64, -v236, v220, v64\n\t"
"v_fma_f32 v65, -v236, v222, v65\n\t"
"v_mfma_f32_16x16x32_bf16 v[92:95], v[208:211], v[116:119], v[92:95]\n\t"
"ds_read_b128 v[116:119], v37 offset:57344\n\t"
"v_mfma_f32_16x16x32_bf16 v[96:99], v[204:207], v[120:123], v[96:99]\n\t"
"v_fma_f32 v72, -v237, v220, v72\n\t"
"v_fma_f32 v73, -v237, v222, v73\n\t"
"v_mfma_f32_16x16x32_bf16 v[100:103], v[208:211], v[120:123], v[100:103]\n\t"
"ds_read_b128 v[120:123], v37 offset:59392\n\t"
"s_waitcnt vmcnt(16)\n\t"
"v_mfma_f32_16x16x32_bf16 v[64:67], v[124:127], v[104:107], v[64:67]\n\t"
"v_fma_f32 v80, -v238, v220, v80\n\t"
"v_fma_f32 v81, -v238, v222, v81\n\t"
"v_mfma_f32_16x16x32_bf16 v[68:71], v[128:131], v[104:107], v[68:71]\n\t"
"ds_read_b128 v[104:107], v38 offset:0\n\t"
"s_waitcnt lgkmcnt(3)\n\t"
"v_mfma_f32_16x16x32_bf16 v[72:75], v[124:127], v[108:111], v[72:75]\n\t"
"v_fma_f32 v88, -v239, v220, v88\n\t"
"v_fma_f32 v89, -v239, v222, v89\n\t"
"v_mfma_f32_16x16x32_bf16 v[76:79], v[128:131], v[108:111], v[76:79]\n\t"
"ds_read_b128 v[108:111], v38 offset:2048\n\t"
"v_mfma_f32_16x16x32_bf16 v[80:83], v[124:127], v[112:115], v[80:83]\n\t"
"v_fma_f32 v96, -v240, v220, v96\n\t"
"v_fma_f32 v97, -v240, v222, v97\n\t"
"v_mfma_f32_16x16x32_bf16 v[84:87], v[128:131], v[112:115], v[84:87]\n\t"
"ds_read_b128 v[112:115], v38 offset:4096\n\t"
"s_waitcnt lgkmcnt(3)\n\t"
"v_mfma_f32_16x16x32_bf16 v[88:91], v[124:127], v[116:119], v[88:91]\n\t"
"v_fma_f32 v66, -v236, v224, v66\n\t"
"v_fma_f32 v67, -v236, v226, v67\n\t"
"v_mfma_f32_16x16x32_bf16 v[92:95], v[128:131], v[116:119], v[92:95]\n\t"
"ds_read_b128 v[116:119], v38 offset:6144\n\t"
"v_mfma_f32_16x16x32_bf16 v[96:99], v[124:127], v[120:123], v[96:99]\n\t"
"v_fma_f32 v74, -v237, v224, v74\n\t"
"v_fma_f32 v75, -v237, v226, v75\n\t"
"v_mfma_f32_16x16x32_bf16 v[100:103], v[128:131], v[120:123], v[100:103]\n\t"
"ds_read_b128 v[120:123], v38 offset:8192\n\t"
"s_waitcnt vmcnt(14) lgkmcnt(3)\n\t"
"v_mfma_f32_16x16x32_bf16 v[64:67], v[132:135], v[104:107], v[64:67]\n\t"
"v_fma_f32 v82, -v238, v224, v82\n\t"
"v_fma_f32 v83, -v238, v226, v83\n\t"
"v_mfma_f32_16x16x32_bf16 v[68:71], v[136:139], v[104:107], v[68:71]\n\t"
"ds_read_b128 v[104:107], v39 offset:0\n\t"
"v_mfma_f32_16x16x32_bf16 v[72:75], v[132:135], v[108:111], v[72:75]\n\t"
"v_fma_f32 v90, -v239, v224, v90\n\t"
"v_fma_f32 v91, -v239, v226, v91\n\t"
"v_mfma_f32_16x16x32_bf16 v[76:79], v[136:139], v[108:111], v[76:79]\n\t"
"ds_read_b128 v[108:111], v39 offset:2048\n\t"
"s_waitcnt lgkmcnt(3)\n\t"
"v_mfma_f32_16x16x32_bf16 v[80:83], v[132:135], v[112:115], v[80:83]\n\t"
"v_fma_f32 v98, -v240, v224, v98\n\t"
"v_fma_f32 v99, -v240, v226, v99\n\t"
"v_mfma_f32_16x16x32_bf16 v[84:87], v[136:139], v[112:115], v[84:87]\n\t"
"ds_read_b128 v[112:115], v39 offset:4096\n\t"
"v_mfma_f32_16x16x32_bf16 v[88:91], v[132:135], v[116:119], v[88:91]\n\t"
"v_fma_f32 v68, -v236, v228, v68\n\t"
"v_fma_f32 v69, -v236, v230, v69\n\t"
"v_mfma_f32_16x16x32_bf16 v[92:95], v[136:139], v[116:119], v[92:95]\n\t"
"ds_read_b128 v[116:119], v39 offset:6144\n\t"
"s_waitcnt lgkmcnt(3)\n\t"
"v_mfma_f32_16x16x32_bf16 v[96:99], v[132:135], v[120:123], v[96:99]\n\t"
"v_fma_f32 v76, -v237, v228, v76\n\t"
"v_fma_f32 v77, -v237, v230, v77\n\t"
"v_mfma_f32_16x16x32_bf16 v[100:103], v[136:139], v[120:123], v[100:103]\n\t"
"ds_read_b128 v[120:123], v39 offset:8192\n\t"
"s_waitcnt vmcnt(12)\n\t"
"v_mfma_f32_16x16x32_bf16 v[64:67], v[140:143], v[104:107], v[64:67]\n\t"
"v_fma_f32 v84, -v238, v228, v84\n\t"
"v_fma_f32 v85, -v238, v230, v85\n\t"
"v_mfma_f32_16x16x32_bf16 v[68:71], v[144:147], v[104:107], v[68:71]\n\t"
"ds_read_b128 v[104:107], v38 offset:10240\n\t"
"s_waitcnt lgkmcnt(3)\n\t"
"v_mfma_f32_16x16x32_bf16 v[72:75], v[140:143], v[108:111], v[72:75]\n\t"
"v_fma_f32 v92, -v239, v228, v92\n\t"
"v_fma_f32 v93, -v239, v230, v93\n\t"
"v_mfma_f32_16x16x32_bf16 v[76:79], v[144:147], v[108:111], v[76:79]\n\t"
"ds_read_b128 v[108:111], v38 offset:12288\n\t"
"v_mfma_f32_16x16x32_bf16 v[80:83], v[140:143], v[112:115], v[80:83]\n\t"
"v_fma_f32 v100, -v240, v228, v100\n\t"
"v_fma_f32 v101, -v240, v230, v101\n\t"
"v_mfma_f32_16x16x32_bf16 v[84:87], v[144:147], v[112:115], v[84:87]\n\t"
"ds_read_b128 v[112:115], v38 offset:14336\n\t"
"s_waitcnt lgkmcnt(3)\n\t"
"v_mfma_f32_16x16x32_bf16 v[88:91], v[140:143], v[116:119], v[88:91]\n\t"
"v_fma_f32 v70, -v236, v232, v70\n\t"
"v_fma_f32 v71, -v236, v234, v71\n\t"
"v_mfma_f32_16x16x32_bf16 v[92:95], v[144:147], v[116:119], v[92:95]\n\t"
"ds_read_b128 v[116:119], v38 offset:16384\n\t"
"v_mfma_f32_16x16x32_bf16 v[96:99], v[140:143], v[120:123], v[96:99]\n\t"
"v_fma_f32 v78, -v237, v232, v78\n\t"
"v_fma_f32 v79, -v237, v234, v79\n\t"
"v_mfma_f32_16x16x32_bf16 v[100:103], v[144:147], v[120:123], v[100:103]\n\t"
"ds_read_b128 v[120:123], v38 offset:18432\n\t"
"s_waitcnt vmcnt(10) lgkmcnt(3)\n\t"
"v_mfma_f32_16x16x32_bf16 v[64:67], v[148:151], v[104:107], v[64:67]\n\t"
"v_fma_f32 v86, -v238, v232, v86\n\t"
"v_fma_f32 v87, -v238, v234, v87\n\t"
"v_mfma_f32_16x16x32_bf16 v[68:71], v[152:155], v[104:107], v[68:71]\n\t"
"ds_read_b128 v[104:107], v39 offset:10240\n\t"
"v_mfma_f32_16x16x32_bf16 v[72:75], v[148:151], v[108:111], v[72:75]\n\t"
"v_fma_f32 v94, -v239, v232, v94\n\t"
"v_fma_f32 v95, -v239, v234, v95\n\t"
"v_mfma_f32_16x16x32_bf16 v[76:79], v[152:155], v[108:111], v[76:79]\n\t"
"ds_read_b128 v[108:111], v39 offset:12288\n\t"
"s_waitcnt lgkmcnt(3)\n\t"
"v_mfma_f32_16x16x32_bf16 v[80:83], v[148:151], v[112:115], v[80:83]\n\t"
"v_fma_f32 v102, -v240, v232, v102\n\t"
"v_fma_f32 v103, -v240, v234, v103\n\t"
"v_mfma_f32_16x16x32_bf16 v[84:87], v[152:155], v[112:115], v[84:87]\n\t"
"ds_read_b128 v[112:115], v39 offset:14336\n\t"
"v_mfma_f32_16x16x32_bf16 v[88:91], v[148:151], v[116:119], v[88:91]\n\t"
"v_mfma_f32_16x16x32_bf16 v[92:95], v[152:155], v[116:119], v[92:95]\n\t"
"ds_read_b128 v[116:119], v39 offset:16384\n\t"
"s_waitcnt lgkmcnt(3)\n\t"
"v_mfma_f32_16x16x32_bf16 v[96:99], v[148:151], v[120:123], v[96:99]\n\t"
"v_mfma_f32_16x16x32_bf16 v[100:103], v[152:155], v[120:123], v[100:103]\n\t"
"ds_read_b128 v[120:123], v39 offset:18432\n\t"
"s_waitcnt vmcnt(8)\n\t"
"v_mfma_f32_16x16x32_bf16 v[64:67], v[156:159], v[104:107], v[64:67]\n\t"
"v_mfma_f32_16x16x32_bf16 v[68:71], v[160:163], v[104:107], v[68:71]\n\t"
"ds_read_b128 v[104:107], v38 offset:20480\n\t"
"s_waitcnt lgkmcnt(3)\n\t"
"v_mfma_f32_16x16x32_bf16 v[72:75], v[156:159], v[108:111], v[72:75]\n\t"
"v_mfma_f32_16x16x32_bf16 v[76:79], v[160:163], v[108:111], v[76:79]\n\t"
"ds_read_b128 v[108:111], v39 offset:20480\n\t"
"v_mfma_f32_16x16x32_bf16 v[80:83], v[156:159], v[112:115], v[80:83]\n\t"
"v_mfma_f32_16x16x32_bf16 v[84:87], v[160:163], v[112:115], v[84:87]\n\t"
"ds_read_b128 v[112:115], v38 offset:30720\n\t"
"s_waitcnt lgkmcnt(3)\n\t"
"v_mfma_f32_16x16x32_bf16 v[88:91], v[156:159], v[116:119], v[88:91]\n\t"
"v_mfma_f32_16x16x32_bf16 v[92:95], v[160:163], v[116:119], v[92:95]\n\t"
"ds_read_b128 v[116:119], v39 offset:30720\n\t"
"v_mfma_f32_16x16x32_bf16 v[96:99], v[156:159], v[120:123], v[96:99]\n\t"
"v_mfma_f32_16x16x32_bf16 v[100:103], v[160:163], v[120:123], v[100:103]\n\t"
"ds_read_b128 v[120:123], v38 offset:22528\n\t"
"s_waitcnt vmcnt(6) lgkmcnt(3)\n\t"
"v_mfma_f32_16x16x32_bf16 v[64:67], v[164:167], v[104:107], v[64:67]\n\t"
"v_mfma_f32_16x16x32_bf16 v[68:71], v[168:171], v[104:107], v[68:71]\n\t"
"ds_read_b128 v[104:107], v39 offset:22528\n\t"
"s_waitcnt vmcnt(4)\n\t"
"v_mfma_f32_16x16x32_bf16 v[64:67], v[172:175], v[108:111], v[64:67]\n\t"
"v_mfma_f32_16x16x32_bf16 v[68:71], v[176:179], v[108:111], v[68:71]\n\t"
"ds_read_b128 v[108:111], v38 offset:32768\n\t"
"s_waitcnt vmcnt(2) lgkmcnt(3)\n\t"
"v_mfma_f32_16x16x32_bf16 v[64:67], v[180:183], v[112:115], v[64:67]\n\t"
"v_mfma_f32_16x16x32_bf16 v[68:71], v[184:187], v[112:115], v[68:71]\n\t"
"ds_read_b128 v[112:115], v39 offset:32768\n\t"
"s_waitcnt vmcnt(0)\n\t"
"v_mfma_f32_16x16x32_bf16 v[64:67], v[188:191], v[116:119], v[64:67]\n\t"
"v_mfma_f32_16x16x32_bf16 v[68:71], v[192:195], v[116:119], v[68:71]\n\t"
"ds_read_b128 v[116:119], v38 offset:24576\n\t"
"s_waitcnt lgkmcnt(3)\n\t"
"v_mfma_f32_16x16x32_bf16 v[72:75], v[164:167], v[120:123], v[72:75]\n\t"
"v_mfma_f32_16x16x32_bf16 v[76:79], v[168:171], v[120:123], v[76:79]\n\t"
"ds_read_b128 v[120:123], v39 offset:24576\n\t"
"v_mfma_f32_16x16x32_bf16 v[72:75], v[172:175], v[104:107], v[72:75]\n\t"
"v_mfma_f32_16x16x32_bf16 v[76:79], v[176:179], v[104:107], v[76:79]\n\t"
"ds_read_b128 v[104:107], v38 offset:34816\n\t"
"s_waitcnt lgkmcnt(3)\n\t"
"v_mfma_f32_16x16x32_bf16 v[72:75], v[180:183], v[108:111], v[72:75]\n\t"
"v_mul_f32 v64, v221, v64\n\t"
"v_mul_f32 v65, v223, v65\n\t"
"v_mul_f32 v66, v225, v66\n\t"
"v_mul_f32 v67, v227, v67\n\t"
"v_mfma_f32_16x16x32_bf16 v[76:79], v[184:187], v[108:111], v[76:79]\n\t"
"ds_read_b128 v[108:111], v39 offset:34816\n\t"
"v_mfma_f32_16x16x32_bf16 v[72:75], v[188:191], v[112:115], v[72:75]\n\t"
"v_mul_f32 v68, v229, v68\n\t"
"v_mul_f32 v69, v231, v69\n\t"
"v_mul_f32 v70, v233, v70\n\t"
"v_mul_f32 v71, v235, v71\n\t"
"v_mfma_f32_16x16x32_bf16 v[76:79], v[192:195], v[112:115], v[76:79]\n\t"
"ds_read_b128 v[112:115], v38 offset:26624\n\t"
"s_waitcnt lgkmcnt(3)\n\t"
"v_mfma_f32_16x16x32_bf16 v[80:83], v[164:167], v[116:119], v[80:83]\n\t"
"v_cvt_pk_bf16_f32 v64, v64, v65\n\t"
"v_cvt_pk_bf16_f32 v65, v66, v67\n\t"
"v_cvt_pk_bf16_f32 v66, v68, v69\n\t"
"v_cvt_pk_bf16_f32 v67, v70, v71\n\t"
"v_mfma_f32_16x16x32_bf16 v[84:87], v[168:171], v[116:119], v[84:87]\n\t"
"ds_read_b128 v[116:119], v39 offset:26624\n\t"
"v_mfma_f32_16x16x32_bf16 v[80:83], v[172:175], v[120:123], v[80:83]\n\t"
"buffer_store_dwordx4 v[64:67], v34, %[rs], s40 offen sc1\n\t"
"v_mfma_f32_16x16x32_bf16 v[84:87], v[176:179], v[120:123], v[84:87]\n\t"
"ds_read_b128 v[120:123], v38 offset:36864\n\t"
"s_waitcnt lgkmcnt(3)\n\t"
"v_mfma_f32_16x16x32_bf16 v[80:83], v[180:183], v[104:107], v[80:83]\n\t"
"v_mul_f32 v72, v221, v72\n\t"
"v_mul_f32 v73, v223, v73\n\t"
"v_mul_f32 v74, v225, v74\n\t"
"v_mul_f32 v75, v227, v75\n\t"
"v_mfma_f32_16x16x32_bf16 v[84:87], v[184:187], v[104:107], v[84:87]\n\t"
"ds_read_b128 v[104:107], v39 offset:36864\n\t"
"v_mfma_f32_16x16x32_bf16 v[80:83], v[188:191], v[108:111], v[80:83]\n\t"
"v_mul_f32 v76, v229, v76\n\t"
"v_mul_f32 v77, v231, v77\n\t"
"v_mul_f32 v78, v233, v78\n\t"
"v_mul_f32 v79, v235, v79\n\t"
"v_mfma_f32_16x16x32_bf16 v[84:87], v[192:195], v[108:111], v[84:87]\n\t"
"ds_read_b128 v[108:111], v38 offset:28672\n\t"
"s_waitcnt lgkmcnt(3)\n\t"
"v_mfma_f32_16x16x32_bf16 v[88:91], v[164:167], v[112:115], v[88:91]\n\t"
"v_cvt_pk_bf16_f32 v72, v72, v73\n\t"
"v_cvt_pk_bf16_f32 v73, v74, v75\n\t"
"v_cvt_pk_bf16_f32 v74, v76, v77\n\t"
"v_cvt_pk_bf16_f32 v75, v78, v79\n\t"
"v_mfma_f32_16x16x32_bf16 v[92:95], v[168:171], v[112:115], v[92:95]\n\t"
"ds_read_b128 v[112:115], v39 offset:28672\n\t"
"v_mfma_f32_16x16x32_bf16 v[88:91], v[172:175], v[116:119], v[88:91]\n\t"
"buffer_store_dwordx4 v[72:75], v34, %[rs], s41 offen sc1\n\t"
"v_mfma_f32_16x16x32_bf16 v[92:95], v[176:179], v[116:119], v[92:95]\n\t"
"ds_read_b128 v[116:119], v38 offset:38912\n\t"
"s_waitcnt lgkmcnt(3)\n\t"
"v_mfma_f32_16x16x32_bf16 v[88:91], v[180:183], v[120:123], v[88:91]\n\t"
"v_mul_f32 v80, v221, v80\n\t"
"v_mul_f32 v81, v223, v81\n\t"
"v_mul_f32 v82, v225, v82\n\t"
"v_mul_f32 v83, v227, v83\n\t"
"v_mfma_f32_16x16x32_bf16 v[92:95], v[184:187], v[120:123], v[92:95]\n\t"
"ds_read_b128 v[120:123], v39 offset:38912\n\t"
"v_mfma_f32_16x16x32_bf16 v[88:91], v[188:191], v[104:107], v[88:91]\n\t"
"v_mul_f32 v84, v229, v84\n\t"
"v_mul_f32 v85, v231, v85\n\t"
"v_mul_f32 v86, v233, v86\n\t"
"v_mul_f32 v87, v235, v87\n\t"
"v_mfma_f32_16x16x32_bf16 v[92:95], v[192:195], v[104:107], v[92:95]\n\t"
"s_waitcnt lgkmcnt(2)\n\t"
"v_mfma_f32_16x16x32_bf16 v[96:99], v[164:167], v[108:111], v[96:99]\n\t"
"v_cvt_pk_bf16_f32 v80, v80, v81\n\t"
"v_cvt_pk_bf16_f32 v81, v82, v83\n\t"
"v_cvt_pk_bf16_f32 v82, v84, v85\n\t"
"v_cvt_pk_bf16_f32 v83, v86, v87\n\t"
"v_mfma_f32_16x16x32_bf16 v[100:103], v[168:171], v[108:111], v[100:103]\n\t"
"v_mfma_f32_16x16x32_bf16 v[96:99], v[172:175], v[112:115], v[96:99]\n\t"
"buffer_store_dwordx4 v[80:83], v34, %[rs], s42 offen sc1\n\t"
"v_mfma_f32_16x16x32_bf16 v[100:103], v[176:179], v[112:115], v[100:103]\n\t"
"s_waitcnt lgkmcnt(0)\n\t"
"v_mfma_f32_16x16x32_bf16 v[96:99], v[180:183], v[116:119], v[96:99]\n\t"
"v_mul_f32 v88, v221, v88\n\t"
"v_mul_f32 v89, v223, v89\n\t"
"v_mul_f32 v90, v225, v90\n\t"
"v_mul_f32 v91, v227, v91\n\t"
"v_mfma_f32_16x16x32_bf16 v[100:103], v[184:187], v[116:119], v[100:103]\n\t"
"v_mfma_f32_16x16x32_bf16 v[96:99], v[188:191], v[120:123], v[96:99]\n\t"
"v_mul_f32 v92, v229, v92\n\t"
"v_mul_f32 v93, v231, v93\n\t"
"v_mul_f32 v94, v233, v94\n\t"
"v_mul_f32 v95, v235, v95\n\t"
"v_mfma_f32_16x16x32_bf16 v[100:103], v[192:195], v[120:123], v[100:103]\n\t"
"s_barrier\n\t"
"v_cvt_pk_bf16_f32 v88, v88, v89\n\t"
"v_cvt_pk_bf16_f32 v89, v90, v91\n\t"
"v_cvt_pk_bf16_f32 v90, v92, v93\n\t"
"v_cvt_pk_bf16_f32 v91, v94, v95\n\t"
"buffer_store_dwordx4 v[88:91], v34, %[rs], s43 offen sc1\n\t"
"s_nop 5\n\t"
"v_mul_f32 v96, v221, v96\n\t"
"v_mul_f32 v97, v223, v97\n\t"
"v_mul_f32 v98, v225, v98\n\t"
"v_mul_f32 v99, v227, v99\n\t"
"v_mul_f32 v100, v229, v100\n\t"
"v_mul_f32 v101, v231, v101\n\t"
"v_mul_f32 v102, v233, v102\n\t"
"v_mul_f32 v103, v235, v103\n\t"
"v_cvt_pk_bf16_f32 v96, v96, v97\n\t"
"v_cvt_pk_bf16_f32 v97, v98, v99\n\t"
"v_cvt_pk_bf16_f32 v98, v100, v101\n\t"
"v_cvt_pk_bf16_f32 v99, v102, v103\n\t"
"buffer_store_dwordx4 v[96:99], v34, %[rs], s44 offen sc1\n\t"
"s_waitcnt vmcnt(0)\n\t"
"buffer_store_dwordx2 v[32:33], v44, %[rs], s45 offen sc1\n\t"

                   : "+{s[36:39]}"(a1_S0), "+{s[40:43]}"(a1_S1), "+{s[44:47]}"(a1_S2), "+{v[32:35]}"(a1_V0), "+{v[36:39]}"(a1_V1), "+{v[40:43]}"(a1_V2), "+{v[44:47]}"(a1_V3)
                   : [rs] "s"(rs)
                   : "memory", "scc", "s48","v64","v65","v66","v67","v68","v69","v70","v71","v72","v73","v74","v75","v76","v77","v78","v79","v80","v81","v82","v83","v84","v85","v86","v87","v88","v89","v90","v91","v92","v93","v94","v95","v96","v97","v98","v99","v100","v101","v102","v103","v104","v105","v106","v107","v108","v109","v110","v111","v112","v113","v114","v115","v116","v117","v118","v119","v120","v121","v122","v123","v124","v125","v126","v127","v128","v129","v130","v131","v132","v133","v134","v135","v136","v137","v138","v139","v140","v141","v142","v143","v144","v145","v146","v147","v148","v149","v150","v151","v152","v153","v154","v155","v156","v157","v158","v159","v160","v161","v162","v163","v164","v165","v166","v167","v168","v169","v170","v171","v172","v173","v174","v175","v176","v177","v178","v179","v180","v181","v182","v183","v184","v185","v186","v187","v188","v189","v190","v191","v192","v193","v194","v195","v196","v197","v198","v199","v200","v201","v202","v203","v204","v205","v206","v207","v208","v209","v210","v211","v212","v213","v214","v215","v216","v217","v218","v219","v220","v221","v222","v223","v224","v225","v226","v227","v228","v229","v230","v231","v232","v233","v234","v235","v236","v237","v238","v239","v240","v241","v242","v243","v244","v245","v246","v247","v248","v249", ACLOB);
    }
    bool ab_ok = false;
    if (ly == 0) {
      PHASE_VARS();
      const rsrc_t rC = __builtin_amdgcn_make_buffer_rsrc((void*)((ks >> 1) ? p.cim : p.cre), 0, 2048 * 2048 * 2, 0x00020000);
      const unsigned rowb = (unsigned)(128 * rb2 + 32 * w) * 4096u + (ks & 1) * 2048, lb = w * 32768 + r * 1024;
      unsigned st, sa, t0, t1;
#define C_Q(i) ((i) ^ cb)
#define C_OPS [rs] "s"(rC), [m0] "s"(w * 32768 + C_Q(0) * 4096), [m1] "s"(w * 32768 + C_Q(1) * 4096), [m2] "s"(w * 32768 + C_Q(2) * 4096), [m3] "s"(w * 32768 + C_Q(3) * 4096), \
              [k0] "s"(rowb + C_Q(0) * 32768), [k1] "s"(rowb + C_Q(1) * 32768), [k2] "s"(rowb + C_Q(2) * 32768), [k3] "s"(rowb + C_Q(3) * 32768),                               \
              [x0] "v"(lane16 ^ (C_Q(0) * 64)), [x1] "v"(lane16 ^ (C_Q(1) * 64)), [x2] "v"(lane16 ^ (C_Q(2) * 64)), [x3] "v"(lane16 ^ (C_Q(3) * 64))
#define C_TMP [st] "=&s"(st), [sa] "=&s"(sa), [t0] "=&v"(t0), [t1] "=&v"(t1)
      asm volatile("s_add_u32 %[sa], %[m0], 0\n\t"
"s_add_u32 %[st], %[k0], 0\n\t"
"v_xor_b32 %[t0], 0, %[x0]\n\t"
"s_add_u32 m0, %[sa], 0\n\t"
"s_nop 0\n\t"
"buffer_load_dwordx4 %[t0], %[rs], %[st] offen lds\n\t"
"s_add_u32 %[st], %[st], 4096\n\t"
"v_xor_b32 %[t1], 16, %[x0]\n\t"
"s_add_u32 m0, %[sa], 1024\n\t"
"s_nop 0\n\t"
"buffer_load_dwordx4 %[t1], %[rs], %[st] offen lds\n\t"
"s_add_u32 %[st], %[st], 4096\n\t"
"v_xor_b32 %[t0], 32, %[x0]\n\t"
"s_add_u32 m0, %[sa], 2048\n\t"
"s_nop 0\n\t"
"buffer_load_dwordx4 %[t0], %[rs], %[st] offen lds\n\t"
"s_add_u32 %[st], %[st], 4096\n\t"
"v_xor_b32 %[t1], 48, %[x0]\n\t"
"s_add_u32 m0, %[sa], 3072\n\t"
"s_nop 0\n\t"
"buffer_load_dwordx4 %[t1], %[rs], %[st] offen lds\n\t"
"s_add_u32 %[sa], %[m1], 0\n\t"
"s_add_u32 %[st], %[k1], 0\n\t"
"v_xor_b32 %[t0], 0, %[x1]\n\t"
"s_add_u32 m0, %[sa], 0\n\t"
"s_nop 0\n\t"
"buffer_load_dwordx4 %[t0], %[rs], %[st] offen lds\n\t"
"s_add_u32 %[st], %[st], 4096\n\t"
"v_xor_b32 %[t1], 16, %[x1]\n\t"
"s_add_u32 m0, %[sa], 1024\n\t"
"s_nop 0\n\t"
"buffer_load_dwordx4 %[t1], %[rs], %[st] offen lds\n\t"
"s_add_u32 %[st], %[st], 4096\n\t"
"v_xor_b32 %[t0], 32, %[x1]\n\t"
"s_add_u32 m0, %[sa], 2048\n\t"
"s_nop 0\n\t"
"buffer_load_dwordx4 %[t0], %[rs], %[st] offen lds\n\t"
"s_add_u32 %[st], %[st], 4096\n\t"
"v_xor_b32 %[t1], 48, %[x1]\n\t"
"s_add_u32 m0, %[sa], 3072\n\t"
"s_nop 0\n\t"
"buffer_load_dwordx4 %[t1], %[rs], %[st] offen lds\n\t"
"s_add_u32 %[sa], %[m2], 0\n\t"
"s_add_u32 %[st], %[k2], 0\n\t"
"v_xor_b32 %[t0], 0, %[x2]\n\t"
"s_add_u32 m0, %[sa], 0\n\t"
"s_nop 0\n\t"
"buffer_load_dwordx4 %[t0], %[rs], %[st] offen lds\n\t"
"s_add_u32 %[st], %[st], 4096\n\t"
"v_xor_b32 %[t1], 16, %[x2]\n\t"
"s_add_u32 m0, %[sa], 1024\n\t"
"s_nop 0\n\t"
"buffer_load_dwordx4 %[t1], %[rs], %[st] offen lds\n\t"
"s_add_u32 %[st], %[st], 4096\n\t"
"v_xor_b32 %[t0], 32, %[x2]\n\t"
"s_add_u32 m0, %[sa], 2048\n\t"
"s_nop 0\n\t"
"buffer_load_dwordx4 %[t0], %[rs], %[st] offen lds\n\t"
"s_add_u32 %[st], %[st], 4096\n\t"
"v_xor_b32 %[t1], 48, %[x2]\n\t"
"s_add_u32 m0, %[sa], 3072\n\t"
"s_nop 0\n\t"
"buffer_load_dwordx4 %[t1], %[rs], %[st] offen lds\n\t"
"s_add_u32 %[sa], %[m3], 0\n\t"
"s_add_u32 %[st], %[k3], 0\n\t"
"v_xor_b32 %[t0], 0, %[x3]\n\t"
"s_add_u32 m0, %[sa], 0\n\t"
"s_nop 0\n\t"
"buffer_load_dwordx4 %[t0], %[rs], %[st] offen lds\n\t"
"s_add_u32 %[st], %[st], 4096\n\t"
"v_xor_b32 %[t1], 16, %[x3]\n\t"
"s_add_u32 m0, %[sa], 1024\n\t"
"s_nop 0\n\t"
"buffer_load_dwordx4 %[t1], %[rs], %[st] offen lds\n\t"
"s_add_u32 %[st], %[st], 4096\n\t"
"v_xor_b32 %[t0], 32, %[x3]\n\t"
"s_add_u32 m0, %[sa], 2048\n\t"
"s_nop 0\n\t"
"buffer_load_dwordx4 %[t0], %[rs], %[st] offen lds\n\t"
"s_add_u32 %[st], %[st], 4096\n\t"
"v_xor_b32 %[t1], 48, %[x3]\n\t"
"s_add_u32 m0, %[sa], 3072\n\t"
"s_nop 0\n\t"
"buffer_load_dwordx4 %[t1], %[rs], %[st] offen lds\n\t"
"s_add_u32 %[sa], %[m0], 16384\n\t"
"s_add_u32 %[st], %[k0], 1024\n\t"
"v_xor_b32 %[t0], 0, %[x0]\n\t"
"s_add_u32 m0, %[sa], 0\n\t"
"s_nop 0\n\t"
"buffer_load_dwordx4 %[t0], %[rs], %[st] offen lds\n\t"
"s_add_u32 %[st], %[st], 4096\n\t"
"v_xor_b32 %[t1], 16, %[x0]\n\t"
"s_add_u32 m0, %[sa], 1024\n\t"
"s_nop 0\n\t"
"buffer_load_dwordx4 %[t1], %[rs], %[st] offen lds\n\t"
"s_add_u32 %[st], %[st], 4096\n\t"
"v_xor_b32 %[t0], 32, %[x0]\n\t"
"s_add_u32 m0, %[sa], 2048\n\t"
"s_nop 0\n\t"
"buffer_load_dwordx4 %[t0], %[rs], %[st] offen lds\n\t"
"s_add_u32 %[st], %[st], 4096\n\t"
"v_xor_b32 %[t1], 48, %[x0]\n\t"
"s_add_u32 m0, %[sa], 3072\n\t"
"s_nop 0\n\t"
"buffer_load_dwordx4 %[t1], %[rs], %[st] offen lds\n\t"
"s_add_u32 %[sa], %[m1], 16384\n\t"
"s_add_u32 %[st], %[k1], 1024\n\t"
"v_xor_b32 %[t0], 0, %[x1]\n\t"
"s_add_u32 m0, %[sa], 0\n\t"
"s_nop 0\n\t"
"buffer_load_dwordx4 %[t0], %[rs], %[st] offen lds\n\t"
"s_add_u32 %[st], %[st], 4096\n\t"
"v_xor_b32 %[t1], 16, %[x1]\n\t"
"s_add_u32 m0, %[sa], 1024\n\t"
"s_nop 0\n\t"
"buffer_load_dwordx4 %[t1], %[rs], %[st] offen lds\n\t"
"s_add_u32 %[st], %[st], 4096\n\t"
"v_xor_b32 %[t0], 32, %[x1]\n\t"
"s_add_u32 m0, %[sa], 2048\n\t"
"s_nop 0\n\t"
"buffer_load_dwordx4 %[t0], %[rs], %[st] offen lds\n\t"
"s_add_u32 %[st], %[st], 4096\n\t"
"v_xor_b32 %[t1], 48, %[x1]\n\t"
"s_add_u32 m0, %[sa], 3072\n\t"
"s_nop 0\n\t"
"buffer_load_dwordx4 %[t1], %[rs], %[st] offen lds\n\t"
"s_add_u32 %[sa], %[m2], 16384\n\t"
"s_add_u32 %[st], %[k2], 1024\n\t"
"v_xor_b32 %[t0], 0, %[x2]\n\t"
"s_add_u32 m0, %[sa], 0\n\t"
"s_nop 0\n\t"
"buffer_load_dwordx4 %[t0], %[rs], %[st] offen lds\n\t"
"s_add_u32 %[st], %[st], 4096\n\t"
"v_xor_b32 %[t1], 16, %[x2]\n\t"
"s_add_u32 m0, %[sa], 1024\n\t"
"s_nop 0\n\t"
"buffer_load_dwordx4 %[t1], %[rs], %[st] offen lds\n\t"
"s_add_u32 %[st], %[st], 4096\n\t"
"v_xor_b32 %[t0], 32, %[x2]\n\t"
"s_add_u32 m0, %[sa], 2048\n\t"
"s_nop 0\n\t"
"buffer_load_dwordx4 %[t0], %[rs], %[st] offen lds\n\t"
"s_add_u32 %[st], %[st], 4096\n\t"
"v_xor_b32 %[t1], 48, %[x2]\n\t"
"s_add_u32 m0, %[sa], 3072\n\t"
"s_nop 0\n\t"
"buffer_load_dwordx4 %[t1], %[rs], %[st] offen lds\n\t"
"s_add_u32 %[sa], %[m3], 16384\n\t"
"s_add_u32 %[st], %[k3], 1024\n\t"
"v_xor_b32 %[t0], 0, %[x3]\n\t"
"s_add_u32 m0, %[sa], 0\n\t"
"s_nop 0\n\t"
"buffer_load_dwordx4 %[t0], %[rs], %[st] offen lds\n\t"
"s_add_u32 %[st], %[st], 4096\n\t"
"v_xor_b32 %[t1], 16, %[x3]\n\t"
"s_add_u32 m0, %[sa], 1024\n\t"
"s_nop 0\n\t"
"buffer_load_dwordx4 %[t1], %[rs], %[st] offen lds\n\t"
"s_add_u32 %[st], %[st], 4096\n\t"
"v_xor_b32 %[t0], 32, %[x3]\n\t"
"s_add_u32 m0, %[sa], 2048\n\t"
"s_nop 0\n\t"
"buffer_load_dwordx4 %[t0], %[rs], %[st] offen lds\n\t"
"s_add_u32 %[st], %[st], 4096\n\t"
"v_xor_b32 %[t1], 48, %[x3]\n\t"
"s_add_u32 m0, %[sa], 3072\n\t"
"s_nop 0\n\t"
"buffer_load_dwordx4 %[t1], %[rs], %[st] offen lds\n\t"
 : C_TMP : C_OPS : "memory", "scc");
      PIECE_VARS();
      if (haspiece) {
        const int G = 4 * cblk + w;
        int tl, dbase, jg = 0;
        bool single;
        if (isw1) { const int m = G / 27, rem = G % 27, jj = min(rem / 7, 3); tl = rem - 7 * jj; single = tl == 6; jg = 4 * m + jj; dbase = OFF_W1T + jg * W1T_SL; }
        else { const int db = G / 5; tl = G % 5; single = tl == 4; dbase = OFF_W2T + db * W2T_SL; }
        const int pp = lane & 3, qq = (lane >> 2) & 3;
        const int cg = single ? pp : (((pp & 1) << 1) | (pp >> 1));
        const unsigned la = L_PT + (unsigned)(8 * q + qq) * 128 + w * 32 + cg * 8;
        const int tile = single ? tl : (tl & ~1) + (r >> 3), rho = single ? r : 8 * (tl & 1) + (r & 7);
        const unsigned dv = (unsigned)tile * 1024u + (unsigned)(q * 16 + rho) * 16u;
        v2u lo[5], hi[5];
        asm volatile("ds_read_b64_tr_b16 %0, %10 offset:0\n\tds_read_b64_tr_b16 %1, %10 offset:512\n\tds_read_b64_tr_b16 %2, %10 offset:4096\n\tds_read_b64_tr_b16 %3, %10 offset:4608\n\t"
                     "ds_read_b64_tr_b16 %4, %10 offset:8192\n\tds_read_b64_tr_b16 %5, %10 offset:8704\n\tds_read_b64_tr_b16 %6, %10 offset:12288\n\tds_read_b64_tr_b16 %7, %10 offset:12800\n\t"
                     "ds_read_b64_tr_b16 %8, %10 offset:16384\n\tds_read_b64_tr_b16 %9, %10 offset:16896\n\ts_waitcnt lgkmcnt(0)"
                     : "=&v"(lo[0]), "=&v"(hi[0]), "=&v"(lo[1]), "=&v"(hi[1]), "=&v"(lo[2]), "=&v"(hi[2]), "=&v"(lo[3]), "=&v"(hi[3]), "=&v"(lo[4]), "=&v"(hi[4])
                     : "v"(la) : "memory");
        float csum = 0.f;
#pragma unroll
        for (int s = 0; s < 5; ++s) {
          const int sg = 5 * kq + s;
          const unsigned so = isw1 ? (unsigned)(s == 4 ? 16 + kq : s == 3 ? 12 + kq : 3 * kq + s) * 7168u : (unsigned)((sg >= 27 ? sg - 27 : sg) * 2 + (sg >= 27 ? 1 : 0)) * 5120u;
          st128(v4u{lo[s].x, lo[s].y, hi[s].x, hi[s].y}, rs, sg < 54 ? dv + dbase + so : OOB, 0);
          csum += ((bflo(lo[s].x) + bfhi(lo[s].x)) + (bflo(lo[s].y) + bfhi(lo[s].y))) + ((bflo(hi[s].x) + bfhi(hi[s].x)) + (bflo(hi[s].y) + bfhi(hi[s].y)));
        }
        csum = sum_x32(sum_x16(csum));
        __builtin_amdgcn_raw_buffer_store_b32(__float_as_uint(csum), rs, (isw1 && q == 0) ? (unsigned)(kq * 1792 + jg * 112 + tile * 16 + rho) * 4u : OOB, OFF_CS, SC1);
      }
      v2u pl;
      asm volatile("s_waitcnt vmcnt(16)\n\t"
"ds_read_b128 a[0:3], %[l0] offset:0\n\t"
"ds_read_b128 a[8:11], %[l1] offset:0\n\t"
"ds_read_b128 a[16:19], %[l2] offset:0\n\t"
"ds_read_b128 a[24:27], %[l3] offset:0\n\t"
"ds_read_b128 a[32:35], %[l0] offset:256\n\t"
"ds_read_b128 a[40:43], %[l1] offset:256\n\t"
"ds_read_b128 a[48:51], %[l2] offset:256\n\t"
"ds_read_b128 a[56:59], %[l3] offset:256\n\t"
"ds_read_b128 a[64:67], %[l0] offset:512\n\t"
"ds_read_b128 a[72:75], %[l1] offset:512\n\t"
"ds_read_b128 a[80:83], %[l2] offset:512\n\t"
"ds_read_b128 a[88:91], %[l3] offset:512\n\t"
"ds_read_b128 a[96:99], %[l0] offset:768\n\t"
"ds_read_b128 a[104:107], %[l1] offset:768\n\t"
"ds_read_b128 a[112:115], %[l2] offset:768\n\t"
"ds_read_b128 a[120:123], %[l3] offset:768\n\t"
"s_waitcnt lgkmcnt(0)\n\t"
"s_add_u32 %[sa], %[m0], 0\n\t"
"s_add_u32 %[st], %[k0], 16384\n\t"
"v_xor_b32 %[t0], 0, %[x0]\n\t"
"s_add_u32 m0, %[sa], 0\n\t"
"s_nop 0\n\t"
"buffer_load_dwordx4 %[t0], %[rs], %[st] offen lds\n\t"
"s_add_u32 %[st], %[st], 4096\n\t"
"v_xor_b32 %[t1], 16, %[x0]\n\t"
"s_add_u32 m0, %[sa], 1024\n\t"
"s_nop 0\n\t"
"buffer_load_dwordx4 %[t1], %[rs], %[st] offen lds\n\t"
"s_add_u32 %[st], %[st], 4096\n\t"
"v_xor_b32 %[t0], 32, %[x0]\n\t"
"s_add_u32 m0, %[sa], 2048\n\t"
"s_nop 0\n\t"
"buffer_load_dwordx4 %[t0], %[rs], %[st] offen lds\n\t"
"s_add_u32 %[st], %[st], 4096\n\t"
"v_xor_b32 %[t1], 48, %[x0]\n\t"
"s_add_u32 m0, %[sa], 3072\n\t"
"s_nop 0\n\t"
"buffer_load_dwordx4 %[t1], %[rs], %[st] offen lds\n\t"
"s_add_u32 %[sa], %[m1], 0\n\t"
"s_add_u32 %[st], %[k1], 16384\n\t"
"v_xor_b32 %[t0], 0, %[x1]\n\t"
"s_add_u32 m0, %[sa], 0\n\t"
"s_nop 0\n\t"
"buffer_load_dwordx4 %[t0], %[rs], %[st] offen lds\n\t"
"s_add_u32 %[st], %[st], 4096\n\t"
"v_xor_b32 %[t1], 16, %[x1]\n\t"
"s_add_u32 m0, %[sa], 1024\n\t"
"s_nop 0\n\t"
"buffer_load_dwordx4 %[t1], %[rs], %[st] offen lds\n\t"
"s_add_u32 %[st], %[st], 4096\n\t"
"v_xor_b32 %[t0], 32, %[x1]\n\t"
"s_add_u32 m0, %[sa], 2048\n\t"
"s_nop 0\n\t"
"buffer_load_dwordx4 %[t0], %[rs], %[st] offen lds\n\t"
"s_add_u32 %[st], %[st], 4096\n\t"
"v_xor_b32 %[t1], 48, %[x1]\n\t"
"s_add_u32 m0, %[sa], 3072\n\t"
"s_nop 0\n\t"
"buffer_load_dwordx4 %[t1], %[rs], %[st] offen lds\n\t"
"s_add_u32 %[sa], %[m2], 0\n\t"
"s_add_u32 %[st], %[k2], 16384\n\t"
"v_xor_b32 %[t0], 0, %[x2]\n\t"
"s_add_u32 m0, %[sa], 0\n\t"
"s_nop 0\n\t"
"buffer_load_dwordx4 %[t0], %[rs], %[st] offen lds\n\t"
"s_add_u32 %[st], %[st], 4096\n\t"
"v_xor_b32 %[t1], 16, %[x2]\n\t"
"s_add_u32 m0, %[sa], 1024\n\t"
"s_nop 0\n\t"
"buffer_load_dwordx4 %[t1], %[rs], %[st] offen lds\n\t"
"s_add_u32 %[st], %[st], 4096\n\t"
"v_xor_b32 %[t0], 32, %[x2]\n\t"
"s_add_u32 m0, %[sa], 2048\n\t"
"s_nop 0\n\t"
"buffer_load_dwordx4 %[t0], %[rs], %[st] offen lds\n\t"
"s_add_u32 %[st], %[st], 4096\n\t"
"v_xor_b32 %[t1], 48, %[x2]\n\t"
"s_add_u32 m0, %[sa], 3072\n\t"
"s_nop 0\n\t"
"buffer_load_dwordx4 %[t1], %[rs], %[st] offen lds\n\t"
"s_add_u32 %[sa], %[m3], 0\n\t"
"s_add_u32 %[st], %[k3], 16384\n\t"
"v_xor_b32 %[t0], 0, %[x3]\n\t"
"s_add_u32 m0, %[sa], 0\n\t"
"s_nop 0\n\t"
"buffer_load_dwordx4 %[t0], %[rs], %[st] offen lds\n\t"
"s_add_u32 %[st], %[st], 4096\n\t"
"v_xor_b32 %[t1], 16, %[x3]\n\t"
"s_add_u32 m0, %[sa], 1024\n\t"
"s_nop 0\n\t"
"buffer_load_dwordx4 %[t1], %[rs], %[st] offen lds\n\t"
"s_add_u32 %[st], %[st], 4096\n\t"
"v_xor_b32 %[t0], 32, %[x3]\n\t"
"s_add_u32 m0, %[sa], 2048\n\t"
"s_nop 0\n\t"
"buffer_load_dwordx4 %[t0], %[rs], %[st] offen lds\n\t"
"s_add_u32 %[st], %[st], 4096\n\t"
"v_xor_b32 %[t1], 48, %[x3]\n\t"
"s_add_u32 m0, %[sa], 3072\n\t"
"s_nop 0\n\t"
"buffer_load_dwordx4 %[t1], %[rs], %[st] offen lds\n\t"
"s_waitcnt vmcnt(16)\n\t"
"ds_read_b128 a[128:131], %[l0] offset:16384\n\t"
"ds_read_b128 a[136:139], %[l1] offset:16384\n\t"
"ds_read_b128 a[144:147], %[l2] offset:16384\n\t"
"ds_read_b128 a[152:155], %[l3] offset:16384\n\t"
"ds_read_b128 a[160:163], %[l0] offset:16640\n\t"
"ds_read_b128 a[168:171], %[l1] offset:16640\n\t"
"ds_read_b128 a[176:179], %[l2] offset:16640\n\t"
"ds_read_b128 a[184:187], %[l3] offset:16640\n\t"
"ds_read_b128 a[192:195], %[l0] offset:16896\n\t"
"ds_read_b128 a[200:203], %[l1] offset:16896\n\t"
"ds_read_b128 a[208:211], %[l2] offset:16896\n\t"
"ds_read_b128 a[216:219], %[l3] offset:16896\n\t"
"ds_read_b128 a[224:227], %[l0] offset:17152\n\t"
"ds_read_b128 a[232:235], %[l1] offset:17152\n\t"
"ds_read_b128 a[240:243], %[l2] offset:17152\n\t"
"ds_read_b128 a[248:251], %[l3] offset:17152\n\t"
"s_waitcnt lgkmcnt(0)\n\t"
"s_add_u32 %[sa], %[m0], 16384\n\t"
"s_add_u32 %[st], %[k0], 17408\n\t"
"v_xor_b32 %[t0], 0, %[x0]\n\t"
"s_add_u32 m0, %[sa], 0\n\t"
"s_nop 0\n\t"
"buffer_load_dwordx4 %[t0], %[rs], %[st] offen lds\n\t"
"s_add_u32 %[st], %[st], 4096\n\t"
"v_xor_b32 %[t1], 16, %[x0]\n\t"
"s_add_u32 m0, %[sa], 1024\n\t"
"s_nop 0\n\t"
"buffer_load_dwordx4 %[t1], %[rs], %[st] offen lds\n\t"
"s_add_u32 %[st], %[st], 4096\n\t"
"v_xor_b32 %[t0], 32, %[x0]\n\t"
"s_add_u32 m0, %[sa], 2048\n\t"
"s_nop 0\n\t"
"buffer_load_dwordx4 %[t0], %[rs], %[st] offen lds\n\t"
"s_add_u32 %[st], %[st], 4096\n\t"
"v_xor_b32 %[t1], 48, %[x0]\n\t"
"s_add_u32 m0, %[sa], 3072\n\t"
"s_nop 0\n\t"
"buffer_load_dwordx4 %[t1], %[rs], %[st] offen lds\n\t"
"s_add_u32 %[sa], %[m1], 16384\n\t"
"s_add_u32 %[st], %[k1], 17408\n\t"
"v_xor_b32 %[t0], 0, %[x1]\n\t"
"s_add_u32 m0, %[sa], 0\n\t"
"s_nop 0\n\t"
"buffer_load_dwordx4 %[t0], %[rs], %[st] offen lds\n\t"
"s_add_u32 %[st], %[st], 4096\n\t"
"v_xor_b32 %[t1], 16, %[x1]\n\t"
"s_add_u32 m0, %[sa], 1024\n\t"
"s_nop 0\n\t"
"buffer_load_dwordx4 %[t1], %[rs], %[st] offen lds\n\t"
"s_add_u32 %[st], %[st], 4096\n\t"
"v_xor_b32 %[t0], 32, %[x1]\n\t"
"s_add_u32 m0, %[sa], 2048\n\t"
"s_nop 0\n\t"
"buffer_load_dwordx4 %[t0], %[rs], %[st] offen lds\n\t"
"s_add_u32 %[st], %[st], 4096\n\t"
"v_xor_b32 %[t1], 48, %[x1]\n\t"
"s_add_u32 m0, %[sa], 3072\n\t"
"s_nop 0\n\t"
"buffer_load_dwordx4 %[t1], %[rs], %[st] offen lds\n\t"
"s_add_u32 %[sa], %[m2], 16384\n\t"
"s_add_u32 %[st], %[k2], 17408\n\t"
"v_xor_b32 %[t0], 0, %[x2]\n\t"
"s_add_u32 m0, %[sa], 0\n\t"
"s_nop 0\n\t"
"buffer_load_dwordx4 %[t0], %[rs], %[st] offen lds\n\t"
"s_add_u32 %[st], %[st], 4096\n\t"
"v_xor_b32 %[t1], 16, %[x2]\n\t"
"s_add_u32 m0, %[sa], 1024\n\t"
"s_nop 0\n\t"
"buffer_load_dwordx4 %[t1], %[rs], %[st] offen lds\n\t"
"s_add_u32 %[st], %[st], 4096\n\t"
"v_xor_b32 %[t0], 32, %[x2]\n\t"
"s_add_u32 m0, %[sa], 2048\n\t"
"s_nop 0\n\t"
"buffer_load_dwordx4 %[t0], %[rs], %[st] offen lds\n\t"
"s_add_u32 %[st], %[st], 4096\n\t"
"v_xor_b32 %[t1], 48, %[x2]\n\t"
"s_add_u32 m0, %[sa], 3072\n\t"
"s_nop 0\n\t"
"buffer_load_dwordx4 %[t1], %[rs], %[st] offen lds\n\t"
"s_add_u32 %[sa], %[m3], 16384\n\t"
"s_add_u32 %[st], %[k3], 17408\n\t"
"v_xor_b32 %[t0], 0, %[x3]\n\t"
"s_add_u32 m0, %[sa], 0\n\t"
"s_nop 0\n\t"
"buffer_load_dwordx4 %[t0], %[rs], %[st] offen lds\n\t"
"s_add_u32 %[st], %[st], 4096\n\t"
"v_xor_b32 %[t1], 16, %[x3]\n\t"
"s_add_u32 m0, %[sa], 1024\n\t"
"s_nop 0\n\t"
"buffer_load_dwordx4 %[t1], %[rs], %[st] offen lds\n\t"
"s_add_u32 %[st], %[st], 4096\n\t"
"v_xor_b32 %[t0], 32, %[x3]\n\t"
"s_add_u32 m0, %[sa], 2048\n\t"
"s_nop 0\n\t"
"buffer_load_dwordx4 %[t0], %[rs], %[st] offen lds\n\t"
"s_add_u32 %[st], %[st], 4096\n\t"
"v_xor_b32 %[t1], 48, %[x3]\n\t"
"s_add_u32 m0, %[sa], 3072\n\t"
"s_nop 0\n\t"
"buffer_load_dwordx4 %[t1], %[rs], %[st] offen lds\n\t"
"s_waitcnt vmcnt(16)\n\t"
"ds_read_b128 a[4:7], %[l0] offset:0\n\t"
"ds_read_b128 a[12:15], %[l1] offset:0\n\t"
"ds_read_b128 a[20:23], %[l2] offset:0\n\t"
"ds_read_b128 a[28:31], %[l3] offset:0\n\t"
"ds_read_b128 a[36:39], %[l0] offset:256\n\t"
"ds_read_b128 a[44:47], %[l1] offset:256\n\t"
"ds_read_b128 a[52:55], %[l2] offset:256\n\t"
"ds_read_b128 a[60:63], %[l3] offset:256\n\t"
"ds_read_b128 a[68:71], %[l0] offset:512\n\t"
"ds_read_b128 a[76:79], %[l1] offset:512\n\t"
"ds_read_b128 a[84:87], %[l2] offset:512\n\t"
"ds_read_b128 a[92:95], %[l3] offset:512\n\t"
"ds_read_b128 a[100:103], %[l0] offset:768\n\t"
"ds_read_b128 a[108:111], %[l1] offset:768\n\t"
"ds_read_b128 a[116:119], %[l2] offset:768\n\t"
"ds_read_b128 a[124:127], %[l3] offset:768\n\t"
"buffer_load_dwordx2 %[pl], %[vf], %[rw], 0 offen sc1\n\t"
"s_waitcnt vmcnt(1)\n\t"
"ds_read_b128 a[132:135], %[l0] offset:16384\n\t"
"ds_read_b128 a[140:143], %[l1] offset:16384\n\t"
"ds_read_b128 a[148:151], %[l2] offset:16384\n\t"
"ds_read_b128 a[156:159], %[l3] offset:16384\n\t"
"ds_read_b128 a[164:167], %[l0] offset:16640\n\t"
"ds_read_b128 a[172:175], %[l1] offset:16640\n\t"
"ds_read_b128 a[180:183], %[l2] offset:16640\n\t"
"ds_read_b128 a[188:191], %[l3] offset:16640\n\t"
"ds_read_b128 a[196:199], %[l0] offset:16896\n\t"
"ds_read_b128 a[204:207], %[l1] offset:16896\n\t"
"ds_read_b128 a[212:215], %[l2] offset:16896\n\t"
"ds_read_b128 a[220:223], %[l3] offset:16896\n\t"
"ds_read_b128 a[228:231], %[l0] offset:17152\n\t"
"ds_read_b128 a[236:239], %[l1] offset:17152\n\t"
"ds_read_b128 a[244:247], %[l2] offset:17152\n\t"
"ds_read_b128 a[252:255], %[l3] offset:17152\n\t"
"s_waitcnt vmcnt(0) lgkmcnt(0)\n\t"
"s_barrier\n\t"

                   : C_TMP, [pl] "=&v"(pl)
                   : C_OPS, [l0] "v"(lb + ((0 + q) ^ r) * 16), [l1] "v"(lb + ((4 + q) ^ r) * 16), [l2] "v"(lb + ((8 + q) ^ r) * 16), [l3] "v"(lb + ((12 + q) ^ r) * 16), [rw] "s"(rs),
                     [vf] "v"(fline(0, FL_AB, ((ks * 4 + cb) * 4 + w) * 2 + half2) + (lane & 15) * 8)
                   : "memory", "scc", ACLOB);
      ab_ok = __all(pl.x == ep0 && pl.y == ep1);
      if (tid == 0) __builtin_amdgcn_raw_buffer_store_b64(v2u{ep0, ep1}, rs, g * 8, OFF_INIT, SC1);
    }
    bool xa_ok = false;
    {
      PHASE_VARS();
      f32x16 late;
      v8u sel;
      const v2u ev = {ep0, ep1};
      const int pt = 4 * rb2 + w, cA = (ks - w) & 3;
      const unsigned flp = fline(ly, FL_P, ((rb2 * 4 + cb) * 4 + w) * 2);
      const unsigned ub = (unsigned)(pt * 80 + 20 * cb) * 512u;
      {
        const unsigned one = (q == (r >> 2)) ? ((r & 1) ? 0x3f800000u : 0x00003f80u) : 0u;
        const unsigned lo = (r & 2) ? 0u : one, hi = (r & 2) ? one : 0u;
        sel[0] = lo; sel[1] = hi; sel[2] = 0u; sel[3] = 0u; sel[4] = 0u; sel[5] = 0u; sel[6] = lo; sel[7] = hi;
      }
      const unsigned a2_sa = OFF_ABT + ly * ABT_B + ((ks >> 1) * 20 + cb * 5) * 131072 + (ks & 1) * 65536;
      const unsigned a2_so = OFF_PART + (ly * 4 + ks) * XU_B + ub, a2_sw = OFF_W1T + c16 * W1T_SL, a2_sm = w * 4096, a2_sk = cA + 1, a2_sf = flp + w * 32;
      const unsigned a2_xa = OFF_XA + ly * XU_B + ub + cA * 2048;
#define PART_O(j) (OFF_PART + (ly * 4 + ((ks + (j)) & 3)) * XU_B + ub)
      const unsigned a2_p0 = PART_O(1) + cA * 2048, a2_p1 = PART_O(2) + cA * 2048, a2_p2 = PART_O(3) + cA * 2048;
      const unsigned a2_vd = w * 4096 + lane16, a2_vp = q * 512 + r * 16, a2_vfs = lane == 0 ? ks * 8u : OOB, a2_vfl = flp + ks * 32 + (lane & 3) * 8, a2_v4 = w == ks ? OOB : a2_vp;
      const unsigned a2_xo = OFF_X1 + ly * XU_B + ub + cA * 2048, a2_to = OFF_ST1 + ly * ST1_B + (unsigned)((cb * 5 + cA) * 2048 + pt * 32) * 8u;
      const unsigned a2_fb = flp + 128, a2_fx = fline(ly, FL_XA, pt * 2) + (cb * 4 + cA) * 8;
      const unsigned a2_vi = OFF_INIT + lane * 32, a2_vt = hh ? OOB : j32 * 8u,  a2_v2 = lane < 2 ? lane * 128u : OOB;
      v4u a2_S0 = {(unsigned)(a2_sa), (unsigned)(a2_so), (unsigned)(a2_sw), (unsigned)(a2_sm)}; v4u a2_S1 = {(unsigned)(a2_sk), (unsigned)(a2_sf), (unsigned)(a2_xa), (unsigned)(a2_p0)}; v4u a2_S2 = {(unsigned)(a2_p1), (unsigned)(a2_p2), (unsigned)(a2_xo), (unsigned)(a2_to)}; v4u a2_S3 = {(unsigned)(a2_fb), (unsigned)(a2_fx), (unsigned)(a2_fx), (unsigned)(a2_fx)}; v4u a2_V0 = {ep0, ep1, (unsigned)(a2_vd), (unsigned)(a2_vp)}; v4u a2_V1 = {(unsigned)(a2_vfs), (unsigned)(a2_vfl), (unsigned)(a2_v4), (unsigned)(a2_vi)}; v4u a2_V2 = {(unsigned)(a2_vt), (unsigned)(a2_v2), (unsigned)(a2_v2), (unsigned)(a2_v2)};
      asm volatile("" : "+{s[36:39]}"(a2_S0), "+{s[40:43]}"(a2_S1), "+{s[44:47]}"(a2_S2), "+{s[48:51]}"(a2_S3));
      asm volatile("" : "+{v[32:35]}"(a2_V0), "+{v[36:39]}"(a2_V1), "+{v[40:43]}"(a2_V2));
      asm volatile("" : "+{v[246:253]}"(sel));
      if (!ab_ok) poll_spin((lane & 15) * 8, rs, fline(ly, FL_AB, ((ks * 4 + cb) * 4 + w) * 2 + half2), ev);
      asm volatile("s_add_u32 s52, s36, 0\n\t"
"s_add_u32 m0, s39, 114688\n\t"
"s_nop 0\n\t"
"buffer_load_dwordx4 v34, %[rs], s52 offen offset:0 sc1 lds\n\t"
"buffer_load_dwordx4 v34, %[rs], s52 offen offset:1024 sc1 lds\n\t"
"buffer_load_dwordx4 v34, %[rs], s52 offen offset:2048 sc1 lds\n\t"
"buffer_load_dwordx4 v34, %[rs], s52 offen offset:3072 sc1 lds\n\t"
"s_add_u32 s52, s36, 16384\n\t"
"s_add_u32 m0, s39, 131072\n\t"
"s_nop 0\n\t"
"buffer_load_dwordx4 v34, %[rs], s52 offen offset:0 sc1 lds\n\t"
"buffer_load_dwordx4 v34, %[rs], s52 offen offset:1024 sc1 lds\n\t"
"buffer_load_dwordx4 v34, %[rs], s52 offen offset:2048 sc1 lds\n\t"
"buffer_load_dwordx4 v34, %[rs], s52 offen offset:3072 sc1 lds\n\t"
"s_mov_b32 s56, 1\n\t"
"s_mov_b32 s59, 0\n\t"
"v_add_u32 v44, 114688, v35\n\t"
"s_waitcnt vmcnt(4)\n\t"
"s_barrier\n\t"
"ds_read_b128 v[96:99], v44 offset:0\n\t"
"ds_read_b128 v[100:103], v44 offset:256\n\t"
"ds_read_b128 v[104:107], v44 offset:2048\n\t"
"ds_read_b128 v[108:111], v44 offset:2304\n\t"
"s_waitcnt lgkmcnt(2)\n\t"
"v_mfma_f32_16x16x32_bf16 v[64:67], v[96:99], a[0:3], 0\n\t"
"s_add_u32 s52, s36, 32768\n\t"
"s_add_u32 m0, s39, 49152\n\t"
"v_mfma_f32_16x16x32_bf16 v[68:71], v[96:99], a[4:7], 0\n\t"
"ds_read_b128 v[96:99], v44 offset:4096\n\t"
"v_mfma_f32_16x16x32_bf16 v[76:79], v[100:103], a[4:7], 0\n\t"
"buffer_load_dwordx4 v34, %[rs], s52 offen offset:0 sc1 lds\n\t"
"v_mfma_f32_16x16x32_bf16 v[72:75], v[100:103], a[0:3], 0\n\t"
"ds_read_b128 v[100:103], v44 offset:4352\n\t"
"s_waitcnt lgkmcnt(2)\n\t"
"v_mfma_f32_16x16x32_bf16 v[64:67], v[104:107], a[8:11], v[64:67]\n\t"
"v_mfma_f32_16x16x32_bf16 v[68:71], v[104:107], a[12:15], v[68:71]\n\t"
"ds_read_b128 v[104:107], v44 offset:6144\n\t"
"v_mfma_f32_16x16x32_bf16 v[76:79], v[108:111], a[12:15], v[76:79]\n\t"
"buffer_load_dwordx4 v34, %[rs], s52 offen offset:1024 sc1 lds\n\t"
"v_mfma_f32_16x16x32_bf16 v[72:75], v[108:111], a[8:11], v[72:75]\n\t"
"ds_read_b128 v[108:111], v44 offset:6400\n\t"
"s_waitcnt lgkmcnt(2)\n\t"
"v_mfma_f32_16x16x32_bf16 v[64:67], v[96:99], a[16:19], v[64:67]\n\t"
"v_mfma_f32_16x16x32_bf16 v[68:71], v[96:99], a[20:23], v[68:71]\n\t"
"ds_read_b128 v[96:99], v44 offset:8192\n\t"
"v_mfma_f32_16x16x32_bf16 v[76:79], v[100:103], a[20:23], v[76:79]\n\t"
"buffer_load_dwordx4 v34, %[rs], s52 offen offset:2048 sc1 lds\n\t"
"v_mfma_f32_16x16x32_bf16 v[72:75], v[100:103], a[16:19], v[72:75]\n\t"
"ds_read_b128 v[100:103], v44 offset:8448\n\t"
"s_waitcnt lgkmcnt(2)\n\t"
"v_mfma_f32_16x16x32_bf16 v[64:67], v[104:107], a[24:27], v[64:67]\n\t"
"v_mfma_f32_16x16x32_bf16 v[68:71], v[104:107], a[28:31], v[68:71]\n\t"
"ds_read_b128 v[104:107], v44 offset:10240\n\t"
"v_mfma_f32_16x16x32_bf16 v[76:79], v[108:111], a[28:31], v[76:79]\n\t"
"buffer_load_dwordx4 v34, %[rs], s52 offen offset:3072 sc1 lds\n\t"
"v_mfma_f32_16x16x32_bf16 v[72:75], v[108:111], a[24:27], v[72:75]\n\t"
"ds_read_b128 v[108:111], v44 offset:10496\n\t"
"s_waitcnt lgkmcnt(2)\n\t"
"v_mfma_f32_16x16x32_bf16 v[64:67], v[96:99], a[32:35], v[64:67]\n\t"
"s_add_u32 s52, s36, 49152\n\t"
"s_add_u32 m0, s39, 65536\n\t"
"v_mfma_f32_16x16x32_bf16 v[68:71], v[96:99], a[36:39], v[68:71]\n\t"
"ds_read_b128 v[96:99], v44 offset:12288\n\t"
"v_mfma_f32_16x16x32_bf16 v[76:79], v[100:103], a[36:39], v[76:79]\n\t"
"buffer_load_dwordx4 v34, %[rs], s52 offen offset:0 sc1 lds\n\t"
"v_mfma_f32_16x16x32_bf16 v[72:75], v[100:103], a[32:35], v[72:75]\n\t"
"ds_read_b128 v[100:103], v44 offset:12544\n\t"
"s_waitcnt lgkmcnt(2)\n\t"
"v_mfma_f32_16x16x32_bf16 v[64:67], v[104:107], a[40:43], v[64:67]\n\t"
"s_mov_b32 s57, 131072\n\t"
"v_mfma_f32_16x16x32_bf16 v[68:71], v[104:107], a[44:47], v[68:71]\n\t"
"ds_read_b128 v[104:107], v44 offset:14336\n\t"
"v_mfma_f32_16x16x32_bf16 v[76:79], v[108:111], a[44:47], v[76:79]\n\t"
"buffer_load_dwordx4 v34, %[rs], s52 offen offset:1024 sc1 lds\n\t"
"v_mfma_f32_16x16x32_bf16 v[72:75], v[108:111], a[40:43], v[72:75]\n\t"
"ds_read_b128 v[108:111], v44 offset:14592\n\t"
"s_waitcnt lgkmcnt(2)\n\t"
"v_mfma_f32_16x16x32_bf16 v[64:67], v[96:99], a[48:51], v[64:67]\n\t"
"s_waitcnt vmcnt(6)\n\t"
"s_barrier\n\t"
"v_add_u32 v44, s57, v35\n\t"
"v_mfma_f32_16x16x32_bf16 v[68:71], v[96:99], a[52:55], v[68:71]\n\t"
"ds_read_b128 v[96:99], v44 offset:0\n\t"
"v_mfma_f32_16x16x32_bf16 v[76:79], v[100:103], a[52:55], v[76:79]\n\t"
"v_mfma_f32_16x16x32_bf16 v[72:75], v[100:103], a[48:51], v[72:75]\n\t"
"ds_read_b128 v[100:103], v44 offset:256\n\t"
"s_waitcnt lgkmcnt(2)\n\t"
"v_mfma_f32_16x16x32_bf16 v[64:67], v[104:107], a[56:59], v[64:67]\n\t"
"buffer_load_dwordx4 v34, %[rs], s52 offen offset:2048 sc1 lds\n\t"
"v_mfma_f32_16x16x32_bf16 v[68:71], v[104:107], a[60:63], v[68:71]\n\t"
"ds_read_b128 v[104:107], v44 offset:2048\n\t"
"v_mfma_f32_16x16x32_bf16 v[76:79], v[108:111], a[60:63], v[76:79]\n\t"
"v_mfma_f32_16x16x32_bf16 v[72:75], v[108:111], a[56:59], v[72:75]\n\t"
"ds_read_b128 v[108:111], v44 offset:2304\n\t"
"s_waitcnt lgkmcnt(2)\n\t"
"v_mfma_f32_16x16x32_bf16 v[64:67], v[96:99], a[64:67], v[64:67]\n\t"
"buffer_load_dwordx4 v34, %[rs], s52 offen offset:3072 sc1 lds\n\t"
"v_mfma_f32_16x16x32_bf16 v[68:71], v[96:99], a[68:71], v[68:71]\n\t"
"ds_read_b128 v[96:99], v44 offset:4096\n\t"
"v_mfma_f32_16x16x32_bf16 v[76:79], v[100:103], a[68:71], v[76:79]\n\t"
"s_add_u32 s52, s36, 131072\n\t"
"s_add_u32 m0, s39, 81920\n\t"
"v_mfma_f32_16x16x32_bf16 v[72:75], v[100:103], a[64:67], v[72:75]\n\t"
"ds_read_b128 v[100:103], v44 offset:4352\n\t"
"s_waitcnt lgkmcnt(2)\n\t"
"v_mfma_f32_16x16x32_bf16 v[64:67], v[104:107], a[72:75], v[64:67]\n\t"
"buffer_load_dwordx4 v34, %[rs], s52 offen offset:0 sc1 lds\n\t"
"v_mfma_f32_16x16x32_bf16 v[68:71], v[104:107], a[76:79], v[68:71]\n\t"
"ds_read_b128 v[104:107], v44 offset:6144\n\t"
"v_mfma_f32_16x16x32_bf16 v[76:79], v[108:111], a[76:79], v[76:79]\n\t"
"v_mfma_f32_16x16x32_bf16 v[72:75], v[108:111], a[72:75], v[72:75]\n\t"
"ds_read_b128 v[108:111], v44 offset:6400\n\t"
"s_waitcnt lgkmcnt(2)\n\t"
"v_mfma_f32_16x16x32_bf16 v[64:67], v[96:99], a[80:83], v[64:67]\n\t"
"buffer_load_dwordx4 v34, %[rs], s52 offen offset:1024 sc1 lds\n\t"
"v_mfma_f32_16x16x32_bf16 v[68:71], v[96:99], a[84:87], v[68:71]\n\t"
"ds_read_b128 v[96:99], v44 offset:8192\n\t"
"v_mfma_f32_16x16x32_bf16 v[76:79], v[100:103], a[84:87], v[76:79]\n\t"
"v_mfma_f32_16x16x32_bf16 v[72:75], v[100:103], a[80:83], v[72:75]\n\t"
"ds_read_b128 v[100:103], v44 offset:8448\n\t"
"s_waitcnt lgkmcnt(2)\n\t"
"v_mfma_f32_16x16x32_bf16 v[64:67], v[104:107], a[88:91], v[64:67]\n\t"
"buffer_load_dwordx4 v34, %[rs], s52 offen offset:2048 sc1 lds\n\t"
"v_mfma_f32_16x16x32_bf16 v[68:71], v[104:107], a[92:95], v[68:71]\n\t"
"ds_read_b128 v[104:107], v44 offset:10240\n\t"
"v_mfma_f32_16x16x32_bf16 v[76:79], v[108:111], a[92:95], v[76:79]\n\t"
"v_mfma_f32_16x16x32_bf16 v[72:75], v[108:111], a[88:91], v[72:75]\n\t"
"ds_read_b128 v[108:111], v44 offset:10496\n\t"
"s_waitcnt lgkmcnt(2)\n\t"
"v_mfma_f32_16x16x32_bf16 v[64:67], v[96:99], a[96:99], v[64:67]\n\t"
"buffer_load_dwordx4 v34, %[rs], s52 offen offset:3072 sc1 lds\n\t"
"v_mfma_f32_16x16x32_bf16 v[68:71], v[96:99], a[100:103], v[68:71]\n\t"
"ds_read_b128 v[96:99], v44 offset:12288\n\t"
"v_mfma_f32_16x16x32_bf16 v[76:79], v[100:103], a[100:103], v[76:79]\n\t"
"s_add_u32 s52, s36, 147456\n\t"
"s_add_u32 m0, s39, 98304\n\t"
"v_mfma_f32_16x16x32_bf16 v[72:75], v[100:103], a[96:99], v[72:75]\n\t"
"ds_read_b128 v[100:103], v44 offset:12544\n\t"
"s_waitcnt lgkmcnt(2)\n\t"
"v_mfma_f32_16x16x32_bf16 v[64:67], v[104:107], a[104:107], v[64:67]\n\t"
"buffer_load_dwordx4 v34, %[rs], s52 offen offset:0 sc1 lds\n\t"
"v_mfma_f32_16x16x32_bf16 v[68:71], v[104:107], a[108:111], v[68:71]\n\t"
"ds_read_b128 v[104:107], v44 offset:14336\n\t"
"v_mfma_f32_16x16x32_bf16 v[76:79], v[108:111], a[108:111], v[76:79]\n\t"
"s_mov_b32 s57, 49152\n\t"
"v_mfma_f32_16x16x32_bf16 v[72:75], v[108:111], a[104:107], v[72:75]\n\t"
"ds_read_b128 v[108:111], v44 offset:14592\n\t"
"s_waitcnt lgkmcnt(2)\n\t"
"v_mfma_f32_16x16x32_bf16 v[64:67], v[96:99], a[112:115], v[64:67]\n\t"
"s_waitcnt vmcnt(9)\n\t"
"s_barrier\n\t"
"v_add_u32 v44, s57, v35\n\t"
"v_mfma_f32_16x16x32_bf16 v[68:71], v[96:99], a[116:119], v[68:71]\n\t"
"ds_read_b128 v[96:99], v44 offset:0\n\t"
"v_mfma_f32_16x16x32_bf16 v[76:79], v[100:103], a[116:119], v[76:79]\n\t"
"buffer_load_dwordx4 v34, %[rs], s52 offen offset:1024 sc1 lds\n\t"
"v_mfma_f32_16x16x32_bf16 v[72:75], v[100:103], a[112:115], v[72:75]\n\t"
"ds_read_b128 v[100:103], v44 offset:256\n\t"
"s_waitcnt lgkmcnt(2)\n\t"
"v_mfma_f32_16x16x32_bf16 v[64:67], v[104:107], a[120:123], v[64:67]\n\t"
"v_mfma_f32_16x16x32_bf16 v[68:71], v[104:107], a[124:127], v[68:71]\n\t"
"ds_read_b128 v[104:107], v44 offset:2048\n\t"
"v_mfma_f32_16x16x32_bf16 v[76:79], v[108:111], a[124:127], v[76:79]\n\t"
"buffer_load_dwordx4 v34, %[rs], s52 offen offset:2048 sc1 lds\n\t"
"v_mfma_f32_16x16x32_bf16 v[72:75], v[108:111], a[120:123], v[72:75]\n\t"
"ds_read_b128 v[108:111], v44 offset:2304\n\t"
"s_waitcnt lgkmcnt(2)\n\t"
"v_mfma_f32_16x16x32_bf16 v[64:67], v[96:99], a[128:131], v[64:67]\n\t"
"v_mfma_f32_16x16x32_bf16 v[68:71], v[96:99], a[132:135], v[68:71]\n\t"
"ds_read_b128 v[96:99], v44 offset:4096\n\t"
"v_mfma_f32_16x16x32_bf16 v[76:79], v[100:103], a[132:135], v[76:79]\n\t"
"buffer_load_dwordx4 v34, %[rs], s52 offen offset:3072 sc1 lds\n\t"
"v_mfma_f32_16x16x32_bf16 v[72:75], v[100:103], a[128:131], v[72:75]\n\t"
"ds_read_b128 v[100:103], v44 offset:4352\n\t"
"s_waitcnt lgkmcnt(2)\n\t"
"v_mfma_f32_16x16x32_bf16 v[64:67], v[104:107], a[136:139], v[64:67]\n\t"
"s_add_u32 s52, s36, 163840\n\t"
"s_add_u32 m0, s39, 114688\n\t"
"v_mfma_f32_16x16x32_bf16 v[68:71], v[104:107], a[140:143], v[68:71]\n\t"
"ds_read_b128 v[104:107], v44 offset:6144\n\t"
"v_mfma_f32_16x16x32_bf16 v[76:79], v[108:111], a[140:143], v[76:79]\n\t"
"buffer_load_dwordx4 v34, %[rs], s52 offen offset:0 sc1 lds\n\t"
"v_mfma_f32_16x16x32_bf16 v[72:75], v[108:111], a[136:139], v[72:75]\n\t"
"ds_read_b128 v[108:111], v44 offset:6400\n\t"
"s_waitcnt lgkmcnt(2)\n\t"
"v_mfma_f32_16x16x32_bf16 v[64:67], v[96:99], a[144:147], v[64:67]\n\t"
"v_mfma_f32_16x16x32_bf16 v[68:71], v[96:99], a[148:151], v[68:71]\n\t"
"ds_read_b128 v[96:99], v44 offset:8192\n\t"
"v_mfma_f32_16x16x32_bf16 v[76:79], v[100:103], a[148:151], v[76:79]\n\t"
"buffer_load_dwordx4 v34, %[rs], s52 offen offset:1024 sc1 lds\n\t"
"v_mfma_f32_16x16x32_bf16 v[72:75], v[100:103], a[144:147], v[72:75]\n\t"
"ds_read_b128 v[100:103], v44 offset:8448\n\t"
"s_waitcnt lgkmcnt(2)\n\t"
"v_mfma_f32_16x16x32_bf16 v[64:67], v[104:107], a[152:155], v[64:67]\n\t"
"v_mfma_f32_16x16x32_bf16 v[68:71], v[104:107], a[156:159], v[68:71]\n\t"
"ds_read_b128 v[104:107], v44 offset:10240\n\t"
"v_mfma_f32_16x16x32_bf16 v[76:79], v[108:111], a[156:159], v[76:79]\n\t"
"buffer_load_dwordx4 v34, %[rs], s52 offen offset:2048 sc1 lds\n\t"
"v_mfma_f32_16x16x32_bf16 v[72:75], v[108:111], a[152:155], v[72:75]\n\t"
"ds_read_b128 v[108:111], v44 offset:10496\n\t"
"s_waitcnt lgkmcnt(2)\n\t"
"v_mfma_f32_16x16x32_bf16 v[64:67], v[96:99], a[160:163], v[64:67]\n\t"
"buffer_load_dwordx4 v[120:123], v39, %[rs], 0 offen sc1\n\t"
"v_mfma_f32_16x16x32_bf16 v[68:71], v[96:99], a[164:167], v[68:71]\n\t"
"ds_read_b128 v[96:99], v44 offset:12288\n\t"
"v_mfma_f32_16x16x32_bf16 v[76:79], v[100:103], a[164:167], v[76:79]\n\t"
"buffer_load_dwordx4 v34, %[rs], s52 offen offset:3072 sc1 lds\n\t"
"v_mfma_f32_16x16x32_bf16 v[72:75], v[100:103], a[160:163], v[72:75]\n\t"
"ds_read_b128 v[100:103], v44 offset:12544\n\t"
"s_waitcnt lgkmcnt(2)\n\t"
"v_mfma_f32_16x16x32_bf16 v[64:67], v[104:107], a[168:171], v[64:67]\n\t"
"buffer_load_dwordx4 v[124:127], v39, %[rs], 0 offen offset:16 sc1\n\t"
"v_mfma_f32_16x16x32_bf16 v[68:71], v[104:107], a[172:175], v[68:71]\n\t"
"ds_read_b128 v[104:107], v44 offset:14336\n\t"
"v_mfma_f32_16x16x32_bf16 v[76:79], v[108:111], a[172:175], v[76:79]\n\t"
"s_mov_b32 s57, 65536\n\t"
"v_mfma_f32_16x16x32_bf16 v[72:75], v[108:111], a[168:171], v[72:75]\n\t"
"ds_read_b128 v[108:111], v44 offset:14592\n\t"
"s_waitcnt lgkmcnt(2)\n\t"
"v_mfma_f32_16x16x32_bf16 v[64:67], v[96:99], a[176:179], v[64:67]\n\t"
"s_waitcnt vmcnt(12)\n\t"
"s_barrier\n\t"
"v_add_u32 v44, s57, v35\n\t"
"v_mfma_f32_16x16x32_bf16 v[68:71], v[96:99], a[180:183], v[68:71]\n\t"
"ds_read_b128 v[96:99], v44 offset:0\n\t"
"v_mfma_f32_16x16x32_bf16 v[76:79], v[100:103], a[180:183], v[76:79]\n\t"
"s_add_u32 s52, s36, 180224\n\t"
"s_add_u32 m0, s39, 131072\n\t"
"v_mfma_f32_16x16x32_bf16 v[72:75], v[100:103], a[176:179], v[72:75]\n\t"
"ds_read_b128 v[100:103], v44 offset:256\n\t"
"s_waitcnt lgkmcnt(2)\n\t"
"v_mfma_f32_16x16x32_bf16 v[64:67], v[104:107], a[184:187], v[64:67]\n\t"
"buffer_load_dwordx4 v34, %[rs], s52 offen offset:0 sc1 lds\n\t"
"v_mfma_f32_16x16x32_bf16 v[68:71], v[104:107], a[188:191], v[68:71]\n\t"
"ds_read_b128 v[104:107], v44 offset:2048\n\t"
"v_mfma_f32_16x16x32_bf16 v[76:79], v[108:111], a[188:191], v[76:79]\n\t"
"v_mfma_f32_16x16x32_bf16 v[72:75], v[108:111], a[184:187], v[72:75]\n\t"
"ds_read_b128 v[108:111], v44 offset:2304\n\t"
"s_waitcnt lgkmcnt(2)\n\t"
"v_mfma_f32_16x16x32_bf16 v[64:67], v[96:99], a[192:195], v[64:67]\n\t"
"v_mfma_f32_16x16x32_bf16 v[68:71], v[96:99], a[196:199], v[68:71]\n\t"
"ds_read_b128 v[96:99], v44 offset:4096\n\t"
"v_mfma_f32_16x16x32_bf16 v[76:79], v[100:103], a[196:199], v[76:79]\n\t"
"buffer_load_dwordx4 v34, %[rs], s52 offen offset:1024 sc1 lds\n\t"
"v_mfma_f32_16x16x32_bf16 v[72:75], v[100:103], a[192:195], v[72:75]\n\t"
"ds_read_b128 v[100:103], v44 offset:4352\n\t"
"s_waitcnt lgkmcnt(2)\n\t"
"v_mfma_f32_16x16x32_bf16 v[64:67], v[104:107], a[200:203], v[64:67]\n\t"
"v_mfma_f32_16x16x32_bf16 v[68:71], v[104:107], a[204:207], v[68:71]\n\t"
"ds_read_b128 v[104:107], v44 offset:6144\n\t"
"v_mfma_f32_16x16x32_bf16 v[76:79], v[108:111], a[204:207], v[76:79]\n\t"
"v_mfma_f32_16x16x32_bf16 v[72:75], v[108:111], a[200:203], v[72:75]\n\t"
"ds_read_b128 v[108:111], v44 offset:6400\n\t"
"s_waitcnt lgkmcnt(2)\n\t"
"v_mfma_f32_16x16x32_bf16 v[64:67], v[96:99], a[208:211], v[64:67]\n\t"
"buffer_load_dwordx4 v34, %[rs], s52 offen offset:2048 sc1 lds\n\t"
"v_mfma_f32_16x16x32_bf16 v[68:71], v[96:99], a[212:215], v[68:71]\n\t"
"ds_read_b128 v[96:99], v44 offset:8192\n\t"
"v_mfma_f32_16x16x32_bf16 v[76:79], v[100:103], a[212:215], v[76:79]\n\t"
"v_mfma_f32_16x16x32_bf16 v[72:75], v[100:103], a[208:211], v[72:75]\n\t"
"ds_read_b128 v[100:103], v44 offset:8448\n\t"
"s_waitcnt lgkmcnt(2)\n\t"
"v_mfma_f32_16x16x32_bf16 v[64:67], v[104:107], a[216:219], v[64:67]\n\t"
"v_mfma_f32_16x16x32_bf16 v[68:71], v[104:107], a[220:223], v[68:71]\n\t"
"ds_read_b128 v[104:107], v44 offset:10240\n\t"
"v_mfma_f32_16x16x32_bf16 v[76:79], v[108:111], a[220:223], v[76:79]\n\t"
"buffer_load_dwordx4 v34, %[rs], s52 offen offset:3072 sc1 lds\n\t"
"v_mfma_f32_16x16x32_bf16 v[72:75], v[108:111], a[216:219], v[72:75]\n\t"
"ds_read_b128 v[108:111], v44 offset:10496\n\t"
"s_waitcnt lgkmcnt(2)\n\t"
"v_mfma_f32_16x16x32_bf16 v[64:67], v[96:99], a[224:227], v[64:67]\n\t"
"s_mov_b32 s57, 81920\n\t"
"v_mfma_f32_16x16x32_bf16 v[68:71], v[96:99], a[228:231], v[68:71]\n\t"
"ds_read_b128 v[96:99], v44 offset:12288\n\t"
"v_mfma_f32_16x16x32_bf16 v[76:79], v[100:103], a[228:231], v[76:79]\n\t"
"v_mfma_f32_16x16x32_bf16 v[72:75], v[100:103], a[224:227], v[72:75]\n\t"
"ds_read_b128 v[100:103], v44 offset:12544\n\t"
"s_waitcnt lgkmcnt(2)\n\t"
"v_mfma_f32_16x16x32_bf16 v[64:67], v[104:107], a[232:235], v[64:67]\n\t"
"v_mfma_f32_16x16x32_bf16 v[68:71], v[104:107], a[236:239], v[68:71]\n\t"
"ds_read_b128 v[104:107], v44 offset:14336\n\t"
"v_mfma_f32_16x16x32_bf16 v[76:79], v[108:111], a[236:239], v[76:79]\n\t"
"s_add_u32 s52, s36, 262144\n\t"
"s_add_u32 m0, s39, 49152\n\t"
"v_mfma_f32_16x16x32_bf16 v[72:75], v[108:111], a[232:235], v[72:75]\n\t"
"ds_read_b128 v[108:111], v44 offset:14592\n\t"
"s_waitcnt lgkmcnt(2)\n\t"
"v_mfma_f32_16x16x32_bf16 v[64:67], v[96:99], a[240:243], v[64:67]\n\t"
"s_waitcnt vmcnt(12)\n\t"
"s_barrier\n\t"
"v_add_u32 v44, s57, v35\n\t"
"v_mfma_f32_16x16x32_bf16 v[68:71], v[96:99], a[244:247], v[68:71]\n\t"
"ds_read_b128 v[96:99], v44 offset:0\n\t"
"v_mfma_f32_16x16x32_bf16 v[76:79], v[100:103], a[244:247], v[76:79]\n\t"
"buffer_load_dwordx4 v34, %[rs], s52 offen offset:0 sc1 lds\n\t"
"v_mfma_f32_16x16x32_bf16 v[72:75], v[100:103], a[240:243], v[72:75]\n\t"
"ds_read_b128 v[100:103], v44 offset:256\n\t"
"s_waitcnt lgkmcnt(2)\n\t"
"v_mfma_f32_16x16x32_bf16 v[198:201], v[104:107], a[248:251], v[64:67]\n\t"
"s_mov_b32 s58, 65536\n\t"
"s_add_u32 s36, s36, 131072\n\t"
"v_mfma_f32_16x16x32_bf16 v[202:205], v[104:107], a[252:255], v[68:71]\n\t"
"ds_read_b128 v[104:107], v44 offset:2048\n\t"
"v_mfma_f32_16x16x32_bf16 v[210:213], v[108:111], a[252:255], v[76:79]\n\t"
"s_waitcnt vmcnt(5)\n\t"
"v_cmp_ne_u64 s[60:61], v[120:121], v[32:33]\n\t"
"v_cmp_ne_u64 s[62:63], v[122:123], v[32:33]\n\t"
"v_cmp_ne_u64 s[64:65], v[124:125], v[32:33]\n\t"
"v_cmp_ne_u64 s[66:67], v[126:127], v[32:33]\n\t"
"s_nop 2\n\t"
"s_or_b64 s[60:61], s[60:61], s[62:63]\n\t"
"s_or_b64 s[64:65], s[64:65], s[66:67]\n\t"
"s_or_b64 s[60:61], s[60:61], s[64:65]\n\t"
"s_cbranch_scc0 L_a2_iok_%=\n\t"
"s_mov_b32 s55, 0x40000\n\t"
"L_a2_ispin_%=:\n\t"
"buffer_load_dwordx4 v[120:123], v39, %[rs], 0 offen sc1\n\t"
"buffer_load_dwordx4 v[124:127], v39, %[rs], 0 offen offset:16 sc1\n\t"
"s_waitcnt vmcnt(0)\n\t"
"v_cmp_ne_u64 s[60:61], v[120:121], v[32:33]\n\t"
"v_cmp_ne_u64 s[62:63], v[122:123], v[32:33]\n\t"
"v_cmp_ne_u64 s[64:65], v[124:125], v[32:33]\n\t"
"v_cmp_ne_u64 s[66:67], v[126:127], v[32:33]\n\t"
"s_sub_u32 s55, s55, 1\n\t"
"s_cbranch_scc1 L_a2_iok_%=\n\t"
"s_nop 2\n\t"
"s_or_b64 s[60:61], s[60:61], s[62:63]\n\t"
"s_or_b64 s[64:65], s[64:65], s[66:67]\n\t"
"s_or_b64 s[60:61], s[60:61], s[64:65]\n\t"
"s_cbranch_scc1 L_a2_ispin_%=\n\t"
"L_a2_iok_%=:\n\t"
"v_mfma_f32_16x16x32_bf16 v[206:209], v[108:111], a[248:251], v[72:75]\n\t"
"ds_read_b128 v[108:111], v44 offset:2304\n\t"
"L_a2_loop_%=:\n\t"
"s_waitcnt lgkmcnt(2)\n\t"
"v_mfma_f32_16x16x32_bf16 v[64:67], v[96:99], a[0:3], 0\n\t"
"buffer_load_dwordx4 v34, %[rs], s52 offen offset:1024 sc1 lds\n\t"
"v_mfma_f32_16x16x32_bf16 v[68:71], v[96:99], a[4:7], 0\n\t"
"ds_read_b128 v[96:99], v44 offset:4096\n\t"
"v_mfma_f32_16x16x32_bf16 v[76:79], v[100:103], a[4:7], 0\n\t"
"s_cmp_eq_u32 s56, s40\n\t"
"s_cselect_b32 s55, 0x80000000, 0\n\t"
"s_cbranch_scc0 L_a2_nokeepl_%=\n\t"
"v_mov_b32 v80, v198\n\t"
"v_mov_b32 v81, v199\n\t"
"v_mov_b32 v82, v200\n\t"
"v_mov_b32 v83, v201\n\t"
"v_mov_b32 v84, v202\n\t"
"v_mov_b32 v85, v203\n\t"
"v_mov_b32 v86, v204\n\t"
"v_mov_b32 v87, v205\n\t"
"v_mov_b32 v88, v206\n\t"
"v_mov_b32 v89, v207\n\t"
"v_mov_b32 v90, v208\n\t"
"v_mov_b32 v91, v209\n\t"
"v_mov_b32 v92, v210\n\t"
"v_mov_b32 v93, v211\n\t"
"v_mov_b32 v94, v212\n\t"
"v_mov_b32 v95, v213\n\t"
"L_a2_nokeepl_%=:\n\t"
"v_mfma_f32_16x16x32_bf16 v[72:75], v[100:103], a[0:3], 0\n\t"
"ds_read_b128 v[100:103], v44 offset:4352\n\t"
"s_waitcnt lgkmcnt(2)\n\t"
"v_mfma_f32_16x16x32_bf16 v[64:67], v[104:107], a[8:11], v[64:67]\n\t"
"v_cvt_pk_bf16_f32 v112, v198, v199\n\t"
"v_cvt_pk_bf16_f32 v113, v200, v201\n\t"
"v_mfma_f32_16x16x32_bf16 v[68:71], v[104:107], a[12:15], v[68:71]\n\t"
"ds_read_b128 v[104:107], v44 offset:6144\n\t"
"v_mfma_f32_16x16x32_bf16 v[76:79], v[108:111], a[12:15], v[76:79]\n\t"
"buffer_load_dwordx4 v34, %[rs], s52 offen offset:2048 sc1 lds\n\t"
"v_mfma_f32_16x16x32_bf16 v[72:75], v[108:111], a[8:11], v[72:75]\n\t"
"ds_read_b128 v[108:111], v44 offset:6400\n\t"
"s_waitcnt lgkmcnt(2)\n\t"
"v_mfma_f32_16x16x32_bf16 v[64:67], v[96:99], a[16:19], v[64:67]\n\t"
"s_mov_b32 s53, s58\n\t"
"s_mov_b32 s58, s57\n\t"
"s_add_u32 s57, s57, 16384\n\t"
"v_mfma_f32_16x16x32_bf16 v[68:71], v[96:99], a[20:23], v[68:71]\n\t"
"ds_read_b128 v[96:99], v44 offset:8192\n\t"
"v_mfma_f32_16x16x32_bf16 v[76:79], v[100:103], a[20:23], v[76:79]\n\t"
"s_cmp_eq_u32 s57, 147456\n\t"
"s_cselect_b32 s57, 49152, s57\n\t"
"v_or_b32 v45, s55, v35\n\t"
"v_mfma_f32_16x16x32_bf16 v[72:75], v[100:103], a[16:19], v[72:75]\n\t"
"ds_read_b128 v[100:103], v44 offset:8448\n\t"
"s_waitcnt lgkmcnt(2)\n\t"
"v_mfma_f32_16x16x32_bf16 v[64:67], v[104:107], a[24:27], v[64:67]\n\t"
"buffer_load_dwordx4 v34, %[rs], s52 offen offset:3072 sc1 lds\n\t"
"v_mfma_f32_16x16x32_bf16 v[68:71], v[104:107], a[28:31], v[68:71]\n\t"
"ds_read_b128 v[104:107], v44 offset:10240\n\t"
"v_mfma_f32_16x16x32_bf16 v[76:79], v[108:111], a[28:31], v[76:79]\n\t"
"s_add_u32 s54, s59, 0\n\t"
"s_add_u32 m0, s39, s54\n\t"
"s_add_u32 s54, s38, s54\n\t"
"v_mfma_f32_16x16x32_bf16 v[72:75], v[108:111], a[24:27], v[72:75]\n\t"
"ds_read_b128 v[108:111], v44 offset:10496\n\t"
"s_waitcnt lgkmcnt(2)\n\t"
"v_mfma_f32_16x16x32_bf16 v[64:67], v[96:99], a[32:35], v[64:67]\n\t"
"v_cvt_pk_bf16_f32 v114, v206, v207\n\t"
"v_cvt_pk_bf16_f32 v115, v208, v209\n\t"
"v_mfma_f32_16x16x32_bf16 v[68:71], v[96:99], a[36:39], v[68:71]\n\t"
"ds_read_b128 v[96:99], v44 offset:12288\n\t"
"v_mfma_f32_16x16x32_bf16 v[76:79], v[100:103], a[36:39], v[76:79]\n\t"
"buffer_load_dwordx4 v34, %[rs], s54 offen sc1 lds\n\t"
"v_mfma_f32_16x16x32_bf16 v[72:75], v[100:103], a[32:35], v[72:75]\n\t"
"ds_read_b128 v[100:103], v44 offset:12544\n\t"
"s_waitcnt lgkmcnt(2)\n\t"
"v_mfma_f32_16x16x32_bf16 v[64:67], v[104:107], a[40:43], v[64:67]\n\t"
"s_add_u32 s52, s36, 147456\n\t"
"s_add_u32 m0, s39, s53\n\t"
"v_mfma_f32_16x16x32_bf16 v[68:71], v[104:107], a[44:47], v[68:71]\n\t"
"ds_read_b128 v[104:107], v44 offset:14336\n\t"
"v_mfma_f32_16x16x32_bf16 v[76:79], v[108:111], a[44:47], v[76:79]\n\t"
"v_cvt_pk_bf16_f32 v116, v202, v203\n\t"
"v_cvt_pk_bf16_f32 v117, v204, v205\n\t"
"v_mfma_f32_16x16x32_bf16 v[72:75], v[108:111], a[40:43], v[72:75]\n\t"
"ds_read_b128 v[108:111], v44 offset:14592\n\t"
"s_waitcnt lgkmcnt(2)\n\t"
"v_mfma_f32_16x16x32_bf16 v[64:67], v[96:99], a[48:51], v[64:67]\n\t"
"s_waitcnt vmcnt(12)\n\t"
"s_barrier\n\t"
"v_add_u32 v44, s57, v35\n\t"
"v_mfma_f32_16x16x32_bf16 v[68:71], v[96:99], a[52:55], v[68:71]\n\t"
"ds_read_b128 v[96:99], v44 offset:0\n\t"
"v_mfma_f32_16x16x32_bf16 v[76:79], v[100:103], a[52:55], v[76:79]\n\t"
"buffer_load_dwordx4 v34, %[rs], s52 offen offset:0 sc1 lds\n\t"
"v_mfma_f32_16x16x32_bf16 v[72:75], v[100:103], a[48:51], v[72:75]\n\t"
"ds_read_b128 v[100:103], v44 offset:256\n\t"
"s_waitcnt lgkmcnt(2)\n\t"
"v_mfma_f32_16x16x32_bf16 v[64:67], v[104:107], a[56:59], v[64:67]\n\t"
"buffer_store_dwordx4 v[112:115], v45, %[rs], s37 offen offset:0 sc1\n\t"
"v_mfma_f32_16x16x32_bf16 v[68:71], v[104:107], a[60:63], v[68:71]\n\t"
"ds_read_b128 v[104:107], v44 offset:2048\n\t"
"v_mfma_f32_16x16x32_bf16 v[76:79], v[108:111], a[60:63], v[76:79]\n\t"
"v_cvt_pk_bf16_f32 v118, v210, v211\n\t"
"v_cvt_pk_bf16_f32 v119, v212, v213\n\t"
"v_mfma_f32_16x16x32_bf16 v[72:75], v[108:111], a[56:59], v[72:75]\n\t"
"ds_read_b128 v[108:111], v44 offset:2304\n\t"
"s_waitcnt lgkmcnt(2)\n\t"
"v_mfma_f32_16x16x32_bf16 v[64:67], v[96:99], a[64:67], v[64:67]\n\t"
"buffer_load_dwordx4 v34, %[rs], s52 offen offset:1024 sc1 lds\n\t"
"v_mfma_f32_16x16x32_bf16 v[68:71], v[96:99], a[68:71], v[68:71]\n\t"
"ds_read_b128 v[96:99], v44 offset:4096\n\t"
"v_mfma_f32_16x16x32_bf16 v[76:79], v[100:103], a[68:71], v[76:79]\n\t"
"buffer_store_dwordx4 v[116:119], v45, %[rs], s37 offen offset:256 sc1\n\t"
"v_mfma_f32_16x16x32_bf16 v[72:75], v[100:103], a[64:67], v[72:75]\n\t"
"ds_read_b128 v[100:103], v44 offset:4352\n\t"
"s_waitcnt lgkmcnt(2)\n\t"
"v_mfma_f32_16x16x32_bf16 v[64:67], v[104:107], a[72:75], v[64:67]\n\t"
"s_add_u32 s37, s37, 2048\n\t"
"v_mfma_f32_16x16x32_bf16 v[68:71], v[104:107], a[76:79], v[68:71]\n\t"
"ds_read_b128 v[104:107], v44 offset:6144\n\t"
"v_mfma_f32_16x16x32_bf16 v[76:79], v[108:111], a[76:79], v[76:79]\n\t"
"buffer_load_dwordx4 v34, %[rs], s52 offen offset:2048 sc1 lds\n\t"
"v_mfma_f32_16x16x32_bf16 v[72:75], v[108:111], a[72:75], v[72:75]\n\t"
"ds_read_b128 v[108:111], v44 offset:6400\n\t"
"s_waitcnt lgkmcnt(2)\n\t"
"v_mfma_f32_16x16x32_bf16 v[64:67], v[96:99], a[80:83], v[64:67]\n\t"
"s_mov_b32 s53, s58\n\t"
"s_mov_b32 s58, s57\n\t"
"s_add_u32 s57, s57, 16384\n\t"
"v_mfma_f32_16x16x32_bf16 v[68:71], v[96:99], a[84:87], v[68:71]\n\t"
"ds_read_b128 v[96:99], v44 offset:8192\n\t"
"v_mfma_f32_16x16x32_bf16 v[76:79], v[100:103], a[84:87], v[76:79]\n\t"
"s_cmp_eq_u32 s57, 147456\n\t"
"s_cselect_b32 s57, 49152, s57\n\t"
"v_mfma_f32_16x16x32_bf16 v[72:75], v[100:103], a[80:83], v[72:75]\n\t"
"ds_read_b128 v[100:103], v44 offset:8448\n\t"
"s_waitcnt lgkmcnt(2)\n\t"
"v_mfma_f32_16x16x32_bf16 v[64:67], v[104:107], a[88:91], v[64:67]\n\t"
"buffer_load_dwordx4 v34, %[rs], s52 offen offset:3072 sc1 lds\n\t"
"v_mfma_f32_16x16x32_bf16 v[68:71], v[104:107], a[92:95], v[68:71]\n\t"
"ds_read_b128 v[104:107], v44 offset:10240\n\t"
"v_mfma_f32_16x16x32_bf16 v[76:79], v[108:111], a[92:95], v[76:79]\n\t"
"s_add_u32 s54, s59, 1024\n\t"
"s_add_u32 m0, s39, s54\n\t"
"s_add_u32 s54, s38, s54\n\t"
"v_mfma_f32_16x16x32_bf16 v[72:75], v[108:111], a[88:91], v[72:75]\n\t"
"ds_read_b128 v[108:111], v44 offset:10496\n\t"
"s_waitcnt lgkmcnt(2)\n\t"
"v_mfma_f32_16x16x32_bf16 v[64:67], v[96:99], a[96:99], v[64:67]\n\t"
"v_mfma_f32_16x16x32_bf16 v[68:71], v[96:99], a[100:103], v[68:71]\n\t"
"ds_read_b128 v[96:99], v44 offset:12288\n\t"
"v_mfma_f32_16x16x32_bf16 v[76:79], v[100:103], a[100:103], v[76:79]\n\t"
"buffer_load_dwordx4 v34, %[rs], s54 offen sc1 lds\n\t"
"v_mfma_f32_16x16x32_bf16 v[72:75], v[100:103], a[96:99], v[72:75]\n\t"
"ds_read_b128 v[100:103], v44 offset:12544\n\t"
"s_waitcnt lgkmcnt(2)\n\t"
"v_mfma_f32_16x16x32_bf16 v[64:67], v[104:107], a[104:107], v[64:67]\n\t"
"s_add_u32 s52, s36, 163840\n\t"
"s_add_u32 m0, s39, s53\n\t"
"v_mfma_f32_16x16x32_bf16 v[68:71], v[104:107], a[108:111], v[68:71]\n\t"
"ds_read_b128 v[104:107], v44 offset:14336\n\t"
"v_mfma_f32_16x16x32_bf16 v[76:79], v[108:111], a[108:111], v[76:79]\n\t"
"v_mfma_f32_16x16x32_bf16 v[72:75], v[108:111], a[104:107], v[72:75]\n\t"
"ds_read_b128 v[108:111], v44 offset:14592\n\t"
"s_waitcnt lgkmcnt(2)\n\t"
"v_mfma_f32_16x16x32_bf16 v[64:67], v[96:99], a[112:115], v[64:67]\n\t"
"s_waitcnt vmcnt(12)\n\t"
"s_barrier\n\t"
"v_add_u32 v44, s57, v35\n\t"
"v_mfma_f32_16x16x32_bf16 v[68:71], v[96:99], a[116:119], v[68:71]\n\t"
"ds_read_b128 v[96:99], v44 offset:0\n\t"
"v_mfma_f32_16x16x32_bf16 v[76:79], v[100:103], a[116:119], v[76:79]\n\t"
"buffer_load_dwordx4 v34, %[rs], s52 offen offset:0 sc1 lds\n\t"
"v_mfma_f32_16x16x32_bf16 v[72:75], v[100:103], a[112:115], v[72:75]\n\t"
"ds_read_b128 v[100:103], v44 offset:256\n\t"
"s_waitcnt lgkmcnt(2)\n\t"
"v_mfma_f32_16x16x32_bf16 v[64:67], v[104:107], a[120:123], v[64:67]\n\t"
"v_mfma_f32_16x16x32_bf16 v[68:71], v[104:107], a[124:127], v[68:71]\n\t"
"ds_read_b128 v[104:107], v44 offset:2048\n\t"
"v_mfma_f32_16x16x32_bf16 v[76:79], v[108:111], a[124:127], v[76:79]\n\t"
"v_mfma_f32_16x16x32_bf16 v[72:75], v[108:111], a[120:123], v[72:75]\n\t"
"ds_read_b128 v[108:111], v44 offset:2304\n\t"
"s_waitcnt lgkmcnt(2)\n\t"
"v_mfma_f32_16x16x32_bf16 v[64:67], v[96:99], a[128:131], v[64:67]\n\t"
"buffer_load_dwordx4 v34, %[rs], s52 offen offset:1024 sc1 lds\n\t"
"v_mfma_f32_16x16x32_bf16 v[68:71], v[96:99], a[132:135], v[68:71]\n\t"
"ds_read_b128 v[96:99], v44 offset:4096\n\t"
"v_mfma_f32_16x16x32_bf16 v[76:79], v[100:103], a[132:135], v[76:79]\n\t"
"v_mfma_f32_16x16x32_bf16 v[72:75], v[100:103], a[128:131], v[72:75]\n\t"
"ds_read_b128 v[100:103], v44 offset:4352\n\t"
"s_waitcnt lgkmcnt(2)\n\t"
"v_mfma_f32_16x16x32_bf16 v[64:67], v[104:107], a[136:139], v[64:67]\n\t"
"v_mfma_f32_16x16x32_bf16 v[68:71], v[104:107], a[140:143], v[68:71]\n\t"
"ds_read_b128 v[104:107], v44 offset:6144\n\t"
"v_mfma_f32_16x16x32_bf16 v[76:79], v[108:111], a[140:143], v[76:79]\n\t"
"buffer_load_dwordx4 v34, %[rs], s52 offen offset:2048 sc1 lds\n\t"
"v_mfma_f32_16x16x32_bf16 v[72:75], v[108:111], a[136:139], v[72:75]\n\t"
"ds_read_b128 v[108:111], v44 offset:6400\n\t"
"s_waitcnt lgkmcnt(2)\n\t"
"v_mfma_f32_16x16x32_bf16 v[64:67], v[96:99], a[144:147], v[64:67]\n\t"
"s_mov_b32 s53, s58\n\t"
"s_mov_b32 s58, s57\n\t"
"s_add_u32 s57, s57, 16384\n\t"
"v_mfma_f32_16x16x32_bf16 v[68:71], v[96:99], a[148:151], v[68:71]\n\t"
"ds_read_b128 v[96:99], v44 offset:8192\n\t"
"v_mfma_f32_16x16x32_bf16 v[76:79], v[100:103], a[148:151], v[76:79]\n\t"
"s_cmp_eq_u32 s57, 147456\n\t"
"s_cselect_b32 s57, 49152, s57\n\t"
"v_mfma_f32_16x16x32_bf16 v[72:75], v[100:103], a[144:147], v[72:75]\n\t"
"ds_read_b128 v[100:103], v44 offset:8448\n\t"
"s_waitcnt lgkmcnt(2)\n\t"
"v_mfma_f32_16x16x32_bf16 v[64:67], v[104:107], a[152:155], v[64:67]\n\t"
"buffer_load_dwordx4 v34, %[rs], s52 offen offset:3072 sc1 lds\n\t"
"v_mfma_f32_16x16x32_bf16 v[68:71], v[104:107], a[156:159], v[68:71]\n\t"
"ds_read_b128 v[104:107], v44 offset:10240\n\t"
"v_mfma_f32_16x16x32_bf16 v[76:79], v[108:111], a[156:159], v[76:79]\n\t"
"s_add_u32 s54, s59, 2048\n\t"
"s_add_u32 m0, s39, s54\n\t"
"s_add_u32 s54, s38, s54\n\t"
"v_mfma_f32_16x16x32_bf16 v[72:75], v[108:111], a[152:155], v[72:75]\n\t"
"ds_read_b128 v[108:111], v44 offset:10496\n\t"
"s_waitcnt lgkmcnt(2)\n\t"
"v_mfma_f32_16x16x32_bf16 v[64:67], v[96:99], a[160:163], v[64:67]\n\t"
"s_waitcnt vmcnt(7)\n\t"
"buffer_store_dwordx2 v[32:33], v36, %[rs], s41 offen sc1\n\t"
"v_mfma_f32_16x16x32_bf16 v[68:71], v[96:99], a[164:167], v[68:71]\n\t"
"ds_read_b128 v[96:99], v44 offset:12288\n\t"
"v_mfma_f32_16x16x32_bf16 v[76:79], v[100:103], a[164:167], v[76:79]\n\t"
"buffer_load_dwordx4 v34, %[rs], s54 offen sc1 lds\n\t"
"v_mfma_f32_16x16x32_bf16 v[72:75], v[100:103], a[160:163], v[72:75]\n\t"
"ds_read_b128 v[100:103], v44 offset:12544\n\t"
"s_waitcnt lgkmcnt(2)\n\t"
"v_mfma_f32_16x16x32_bf16 v[64:67], v[104:107], a[168:171], v[64:67]\n\t"
"s_add_u32 s52, s36, 180224\n\t"
"s_add_u32 m0, s39, s53\n\t"
"v_mfma_f32_16x16x32_bf16 v[68:71], v[104:107], a[172:175], v[68:71]\n\t"
"ds_read_b128 v[104:107], v44 offset:14336\n\t"
"v_mfma_f32_16x16x32_bf16 v[76:79], v[108:111], a[172:175], v[76:79]\n\t"
"v_mfma_f32_16x16x32_bf16 v[72:75], v[108:111], a[168:171], v[72:75]\n\t"
"ds_read_b128 v[108:111], v44 offset:14592\n\t"
"s_waitcnt lgkmcnt(2)\n\t"
"v_mfma_f32_16x16x32_bf16 v[64:67], v[96:99], a[176:179], v[64:67]\n\t"
"s_waitcnt vmcnt(12)\n\t"
"s_barrier\n\t"
"v_add_u32 v44, s57, v35\n\t"
"v_mfma_f32_16x16x32_bf16 v[68:71], v[96:99], a[180:183], v[68:71]\n\t"
"ds_read_b128 v[96:99], v44 offset:0\n\t"
"v_mfma_f32_16x16x32_bf16 v[76:79], v[100:103], a[180:183], v[76:79]\n\t"
"buffer_load_dwordx4 v34, %[rs], s52 offen offset:0 sc1 lds\n\t"
"v_mfma_f32_16x16x32_bf16 v[72:75], v[100:103], a[176:179], v[72:75]\n\t"
"ds_read_b128 v[100:103], v44 offset:256\n\t"
"s_waitcnt lgkmcnt(2)\n\t"
"v_mfma_f32_16x16x32_bf16 v[64:67], v[104:107], a[184:187], v[64:67]\n\t"
"v_mfma_f32_16x16x32_bf16 v[68:71], v[104:107], a[188:191], v[68:71]\n\t"
"ds_read_b128 v[104:107], v44 offset:2048\n\t"
"v_mfma_f32_16x16x32_bf16 v[76:79], v[108:111], a[188:191], v[76:79]\n\t"
"s_add_u32 s41, s41, 32\n\t"
"s_bitset0_b32 s41, 7\n\t"
"v_mfma_f32_16x16x32_bf16 v[72:75], v[108:111], a[184:187], v[72:75]\n\t"
"ds_read_b128 v[108:111], v44 offset:2304\n\t"
"s_waitcnt lgkmcnt(2)\n\t"
"v_mfma_f32_16x16x32_bf16 v[64:67], v[96:99], a[192:195], v[64:67]\n\t"
"buffer_load_dwordx4 v34, %[rs], s52 offen offset:1024 sc1 lds\n\t"
"v_mfma_f32_16x16x32_bf16 v[68:71], v[96:99], a[196:199], v[68:71]\n\t"
"ds_read_b128 v[96:99], v44 offset:4096\n\t"
"v_mfma_f32_16x16x32_bf16 v[76:79], v[100:103], a[196:199], v[76:79]\n\t"
"v_mfma_f32_16x16x32_bf16 v[72:75], v[100:103], a[192:195], v[72:75]\n\t"
"ds_read_b128 v[100:103], v44 offset:4352\n\t"
"s_waitcnt lgkmcnt(2)\n\t"
"v_mfma_f32_16x16x32_bf16 v[64:67], v[104:107], a[200:203], v[64:67]\n\t"
"v_mfma_f32_16x16x32_bf16 v[68:71], v[104:107], a[204:207], v[68:71]\n\t"
"ds_read_b128 v[104:107], v44 offset:6144\n\t"
"v_mfma_f32_16x16x32_bf16 v[76:79], v[108:111], a[204:207], v[76:79]\n\t"
"buffer_load_dwordx4 v34, %[rs], s52 offen offset:2048 sc1 lds\n\t"
"v_mfma_f32_16x16x32_bf16 v[72:75], v[108:111], a[200:203], v[72:75]\n\t"
"ds_read_b128 v[108:111], v44 offset:6400\n\t"
"s_waitcnt lgkmcnt(2)\n\t"
"v_mfma_f32_16x16x32_bf16 v[64:67], v[96:99], a[208:211], v[64:67]\n\t"
"s_mov_b32 s53, s58\n\t"
"s_mov_b32 s58, s57\n\t"
"s_add_u32 s57, s57, 16384\n\t"
"v_mfma_f32_16x16x32_bf16 v[68:71], v[96:99], a[212:215], v[68:71]\n\t"
"ds_read_b128 v[96:99], v44 offset:8192\n\t"
"v_mfma_f32_16x16x32_bf16 v[76:79], v[100:103], a[212:215], v[76:79]\n\t"
"s_cmp_eq_u32 s57, 147456\n\t"
"s_cselect_b32 s57, 49152, s57\n\t"
"v_mfma_f32_16x16x32_bf16 v[72:75], v[100:103], a[208:211], v[72:75]\n\t"
"ds_read_b128 v[100:103], v44 offset:8448\n\t"
"s_waitcnt lgkmcnt(2)\n\t"
"v_mfma_f32_16x16x32_bf16 v[64:67], v[104:107], a[216:219], v[64:67]\n\t"
"buffer_load_dwordx4 v34, %[rs], s52 offen offset:3072 sc1 lds\n\t"
"v_mfma_f32_16x16x32_bf16 v[68:71], v[104:107], a[220:223], v[68:71]\n\t"
"ds_read_b128 v[104:107], v44 offset:10240\n\t"
"v_mfma_f32_16x16x32_bf16 v[76:79], v[108:111], a[220:223], v[76:79]\n\t"
"s_add_u32 s54, s59, 3072\n\t"
"s_add_u32 m0, s39, s54\n\t"
"s_add_u32 s54, s38, s54\n\t"
"v_mfma_f32_16x16x32_bf16 v[72:75], v[108:111], a[216:219], v[72:75]\n\t"
"ds_read_b128 v[108:111], v44 offset:10496\n\t"
"s_waitcnt lgkmcnt(2)\n\t"
"v_mfma_f32_16x16x32_bf16 v[64:67], v[96:99], a[224:227], v[64:67]\n\t"
"s_add_u32 s55, s36, 262144\n\t"
"s_add_u32 s52, s38, s53\n\t"
"v_mfma_f32_16x16x32_bf16 v[68:71], v[96:99], a[228:231], v[68:71]\n\t"
"ds_read_b128 v[96:99], v44 offset:12288\n\t"
"v_mfma_f32_16x16x32_bf16 v[76:79], v[100:103], a[228:231], v[76:79]\n\t"
"buffer_load_dwordx4 v34, %[rs], s54 offen sc1 lds\n\t"
"v_mfma_f32_16x16x32_bf16 v[72:75], v[100:103], a[224:227], v[72:75]\n\t"
"ds_read_b128 v[100:103], v44 offset:12544\n\t"
"s_waitcnt lgkmcnt(2)\n\t"
"v_mfma_f32_16x16x32_bf16 v[64:67], v[104:107], a[232:235], v[64:67]\n\t"
"s_add_u32 m0, s39, s53\n\t"
"v_mfma_f32_16x16x32_bf16 v[68:71], v[104:107], a[236:239], v[68:71]\n\t"
"ds_read_b128 v[104:107], v44 offset:14336\n\t"
"v_mfma_f32_16x16x32_bf16 v[76:79], v[108:111], a[236:239], v[76:79]\n\t"
"s_cmp_lt_u32 s56, 3\n\t"
"s_cselect_b32 s52, s55, s52\n\t"
"v_mfma_f32_16x16x32_bf16 v[72:75], v[108:111], a[232:235], v[72:75]\n\t"
"ds_read_b128 v[108:111], v44 offset:14592\n\t"
"s_waitcnt lgkmcnt(2)\n\t"
"v_mfma_f32_16x16x32_bf16 v[64:67], v[96:99], a[240:243], v[64:67]\n\t"
"s_waitcnt vmcnt(12)\n\t"
"s_barrier\n\t"
"v_add_u32 v44, s57, v35\n\t"
"v_mfma_f32_16x16x32_bf16 v[68:71], v[96:99], a[244:247], v[68:71]\n\t"
"ds_read_b128 v[96:99], v44 offset:0\n\t"
"v_mfma_f32_16x16x32_bf16 v[76:79], v[100:103], a[244:247], v[76:79]\n\t"
"buffer_load_dwordx4 v34, %[rs], s52 offen offset:0 sc1 lds\n\t"
"v_mfma_f32_16x16x32_bf16 v[72:75], v[100:103], a[240:243], v[72:75]\n\t"
"ds_read_b128 v[100:103], v44 offset:256\n\t"
"s_waitcnt lgkmcnt(2)\n\t"
"v_mfma_f32_16x16x32_bf16 v[198:201], v[104:107], a[248:251], v[64:67]\n\t"
"s_add_u32 s59, s59, 16384\n\t"
"s_add_u32 s36, s36, 131072\n\t"
"v_mfma_f32_16x16x32_bf16 v[202:205], v[104:107], a[252:255], v[68:71]\n\t"
"ds_read_b128 v[104:107], v44 offset:2048\n\t"
"v_mfma_f32_16x16x32_bf16 v[210:213], v[108:111], a[252:255], v[76:79]\n\t"
"s_add_u32 s56, s56, 1\n\t"
"s_cmp_lt_u32 s56, 4\n\t"
"v_mfma_f32_16x16x32_bf16 v[206:209], v[108:111], a[248:251], v[72:75]\n\t"
"ds_read_b128 v[108:111], v44 offset:2304\n\t"
"s_cbranch_scc1 L_a2_loop_%=\n\t"
"s_waitcnt lgkmcnt(2)\n\t"
"v_mfma_f32_16x16x32_bf16 v[64:67], v[96:99], a[0:3], 0\n\t"
"buffer_load_dwordx4 v34, %[rs], s52 offen offset:1024 sc1 lds\n\t"
"v_mfma_f32_16x16x32_bf16 v[68:71], v[96:99], a[4:7], 0\n\t"
"ds_read_b128 v[96:99], v44 offset:4096\n\t"
"v_mfma_f32_16x16x32_bf16 v[76:79], v[100:103], a[4:7], 0\n\t"
"s_cmp_eq_u32 s56, s40\n\t"
"s_cselect_b32 s55, 0x80000000, 0\n\t"
"s_cbranch_scc0 L_a2_nokeepz_%=\n\t"
"v_mov_b32 v80, v198\n\t"
"v_mov_b32 v81, v199\n\t"
"v_mov_b32 v82, v200\n\t"
"v_mov_b32 v83, v201\n\t"
"v_mov_b32 v84, v202\n\t"
"v_mov_b32 v85, v203\n\t"
"v_mov_b32 v86, v204\n\t"
"v_mov_b32 v87, v205\n\t"
"v_mov_b32 v88, v206\n\t"
"v_mov_b32 v89, v207\n\t"
"v_mov_b32 v90, v208\n\t"
"v_mov_b32 v91, v209\n\t"
"v_mov_b32 v92, v210\n\t"
"v_mov_b32 v93, v211\n\t"
"v_mov_b32 v94, v212\n\t"
"v_mov_b32 v95, v213\n\t"
"L_a2_nokeepz_%=:\n\t"
"v_mfma_f32_16x16x32_bf16 v[72:75], v[100:103], a[0:3], 0\n\t"
"ds_read_b128 v[100:103], v44 offset:4352\n\t"
"s_waitcnt lgkmcnt(2)\n\t"
"v_mfma_f32_16x16x32_bf16 v[64:67], v[104:107], a[8:11], v[64:67]\n\t"
"v_cvt_pk_bf16_f32 v112, v198, v199\n\t"
"v_cvt_pk_bf16_f32 v113, v200, v201\n\t"
"v_mfma_f32_16x16x32_bf16 v[68:71], v[104:107], a[12:15], v[68:71]\n\t"
"ds_read_b128 v[104:107], v44 offset:6144\n\t"
"v_mfma_f32_16x16x32_bf16 v[76:79], v[108:111], a[12:15], v[76:79]\n\t"
"buffer_load_dwordx4 v34, %[rs], s52 offen offset:2048 sc1 lds\n\t"
"v_mfma_f32_16x16x32_bf16 v[72:75], v[108:111], a[8:11], v[72:75]\n\t"
"ds_read_b128 v[108:111], v44 offset:6400\n\t"
"s_waitcnt lgkmcnt(2)\n\t"
"v_mfma_f32_16x16x32_bf16 v[64:67], v[96:99], a[16:19], v[64:67]\n\t"
"s_mov_b32 s53, s58\n\t"
"s_mov_b32 s58, s57\n\t"
"s_add_u32 s57, s57, 16384\n\t"
"v_mfma_f32_16x16x32_bf16 v[68:71], v[96:99], a[20:23], v[68:71]\n\t"
"ds_read_b128 v[96:99], v44 offset:8192\n\t"
"v_mfma_f32_16x16x32_bf16 v[76:79], v[100:103], a[20:23], v[76:79]\n\t"
"s_cmp_eq_u32 s57, 147456\n\t"
"s_cselect_b32 s57, 49152, s57\n\t"
"v_or_b32 v45, s55, v35\n\t"
"v_mfma_f32_16x16x32_bf16 v[72:75], v[100:103], a[16:19], v[72:75]\n\t"
"ds_read_b128 v[100:103], v44 offset:8448\n\t"
"s_waitcnt lgkmcnt(2)\n\t"
"v_mfma_f32_16x16x32_bf16 v[64:67], v[104:107], a[24:27], v[64:67]\n\t"
"buffer_load_dwordx4 v34, %[rs], s52 offen offset:3072 sc1 lds\n\t"
"v_mfma_f32_16x16x32_bf16 v[68:71], v[104:107], a[28:31], v[68:71]\n\t"
"ds_read_b128 v[104:107], v44 offset:10240\n\t"
"v_mfma_f32_16x16x32_bf16 v[76:79], v[108:111], a[28:31], v[76:79]\n\t"
"v_cvt_pk_bf16_f32 v114, v206, v207\n\t"
"v_cvt_pk_bf16_f32 v115, v208, v209\n\t"
"v_mfma_f32_16x16x32_bf16 v[72:75], v[108:111], a[24:27], v[72:75]\n\t"
"ds_read_b128 v[108:111], v44 offset:10496\n\t"
"s_waitcnt lgkmcnt(2)\n\t"
"v_mfma_f32_16x16x32_bf16 v[64:67], v[96:99], a[32:35], v[64:67]\n\t"
"buffer_store_dwordx4 v[112:115], v45, %[rs], s37 offen offset:0 sc1\n\t"
"v_mfma_f32_16x16x32_bf16 v[68:71], v[96:99], a[36:39], v[68:71]\n\t"
"ds_read_b128 v[96:99], v44 offset:12288\n\t"
"v_mfma_f32_16x16x32_bf16 v[76:79], v[100:103], a[36:39], v[76:79]\n\t"
"v_cvt_pk_bf16_f32 v116, v202, v203\n\t"
"v_cvt_pk_bf16_f32 v117, v204, v205\n\t"
"v_mfma_f32_16x16x32_bf16 v[72:75], v[100:103], a[32:35], v[72:75]\n\t"
"ds_read_b128 v[100:103], v44 offset:12544\n\t"
"s_waitcnt lgkmcnt(2)\n\t"
"v_mfma_f32_16x16x32_bf16 v[64:67], v[104:107], a[40:43], v[64:67]\n\t"
"s_add_u32 s52, s38, s53\n\t"
"s_add_u32 m0, s39, s53\n\t"
"v_mfma_f32_16x16x32_bf16 v[68:71], v[104:107], a[44:47], v[68:71]\n\t"
"ds_read_b128 v[104:107], v44 offset:14336\n\t"
"v_mfma_f32_16x16x32_bf16 v[76:79], v[108:111], a[44:47], v[76:79]\n\t"
"v_cvt_pk_bf16_f32 v118, v210, v211\n\t"
"v_cvt_pk_bf16_f32 v119, v212, v213\n\t"
"v_mfma_f32_16x16x32_bf16 v[72:75], v[108:111], a[40:43], v[72:75]\n\t"
"ds_read_b128 v[108:111], v44 offset:14592\n\t"
"s_waitcnt lgkmcnt(2)\n\t"
"v_mfma_f32_16x16x32_bf16 v[64:67], v[96:99], a[48:51], v[64:67]\n\t"
"s_waitcnt vmcnt(12)\n\t"
"s_barrier\n\t"
"v_add_u32 v44, s57, v35\n\t"
"v_mfma_f32_16x16x32_bf16 v[68:71], v[96:99], a[52:55], v[68:71]\n\t"
"ds_read_b128 v[96:99], v44 offset:0\n\t"
"v_mfma_f32_16x16x32_bf16 v[76:79], v[100:103], a[52:55], v[76:79]\n\t"
"buffer_load_dwordx4 v34, %[rs], s52 offen offset:0 sc1 lds\n\t"
"v_mfma_f32_16x16x32_bf16 v[72:75], v[100:103], a[48:51], v[72:75]\n\t"
"ds_read_b128 v[100:103], v44 offset:256\n\t"
"s_waitcnt lgkmcnt(2)\n\t"
"v_mfma_f32_16x16x32_bf16 v[64:67], v[104:107], a[56:59], v[64:67]\n\t"
"buffer_store_dwordx4 v[116:119], v45, %[rs], s37 offen offset:256 sc1\n\t"
"v_mfma_f32_16x16x32_bf16 v[68:71], v[104:107], a[60:63], v[68:71]\n\t"
"ds_read_b128 v[104:107], v44 offset:2048\n\t"
"v_mfma_f32_16x16x32_bf16 v[76:79], v[108:111], a[60:63], v[76:79]\n\t"
"v_mfma_f32_16x16x32_bf16 v[72:75], v[108:111], a[56:59], v[72:75]\n\t"
"ds_read_b128 v[108:111], v44 offset:2304\n\t"
"s_waitcnt lgkmcnt(2)\n\t"
"v_mfma_f32_16x16x32_bf16 v[64:67], v[96:99], a[64:67], v[64:67]\n\t"
"buffer_load_dwordx4 v34, %[rs], s52 offen offset:1024 sc1 lds\n\t"
"v_mfma_f32_16x16x32_bf16 v[68:71], v[96:99], a[68:71], v[68:71]\n\t"
"ds_read_b128 v[96:99], v44 offset:4096\n\t"
"v_mfma_f32_16x16x32_bf16 v[76:79], v[100:103], a[68:71], v[76:79]\n\t"
"buffer_load_dwordx2 v[120:121], v37, %[rs], 0 offen sc1\n\t"
"v_mfma_f32_16x16x32_bf16 v[72:75], v[100:103], a[64:67], v[72:75]\n\t"
"ds_read_b128 v[100:103], v44 offset:4352\n\t"
"s_waitcnt lgkmcnt(2)\n\t"
"v_mfma_f32_16x16x32_bf16 v[64:67], v[104:107], a[72:75], v[64:67]\n\t"
"s_add_u32 s37, s37, 2048\n\t"
"v_mfma_f32_16x16x32_bf16 v[68:71], v[104:107], a[76:79], v[68:71]\n\t"
"ds_read_b128 v[104:107], v44 offset:6144\n\t"
"v_mfma_f32_16x16x32_bf16 v[76:79], v[108:111], a[76:79], v[76:79]\n\t"
"buffer_load_dwordx4 v34, %[rs], s52 offen offset:2048 sc1 lds\n\t"
"v_mfma_f32_16x16x32_bf16 v[72:75], v[108:111], a[72:75], v[72:75]\n\t"
"ds_read_b128 v[108:111], v44 offset:6400\n\t"
"s_waitcnt lgkmcnt(2)\n\t"
"v_mfma_f32_16x16x32_bf16 v[64:67], v[96:99], a[80:83], v[64:67]\n\t"
"s_mov_b32 s53, s58\n\t"
"s_mov_b32 s58, s57\n\t"
"s_add_u32 s57, s57, 16384\n\t"
"v_mfma_f32_16x16x32_bf16 v[68:71], v[96:99], a[84:87], v[68:71]\n\t"
"ds_read_b128 v[96:99], v44 offset:8192\n\t"
"v_mfma_f32_16x16x32_bf16 v[76:79], v[100:103], a[84:87], v[76:79]\n\t"
"s_cmp_eq_u32 s57, 147456\n\t"
"s_cselect_b32 s57, 49152, s57\n\t"
"v_mfma_f32_16x16x32_bf16 v[72:75], v[100:103], a[80:83], v[72:75]\n\t"
"ds_read_b128 v[100:103], v44 offset:8448\n\t"
"s_waitcnt lgkmcnt(2)\n\t"
"v_mfma_f32_16x16x32_bf16 v[64:67], v[104:107], a[88:91], v[64:67]\n\t"
"buffer_load_dwordx4 v34, %[rs], s52 offen offset:3072 sc1 lds\n\t"
"v_mfma_f32_16x16x32_bf16 v[68:71], v[104:107], a[92:95], v[68:71]\n\t"
"ds_read_b128 v[104:107], v44 offset:10240\n\t"
"v_mfma_f32_16x16x32_bf16 v[76:79], v[108:111], a[92:95], v[76:79]\n\t"
"v_mfma_f32_16x16x32_bf16 v[72:75], v[108:111], a[88:91], v[72:75]\n\t"
"ds_read_b128 v[108:111], v44 offset:10496\n\t"
"s_waitcnt lgkmcnt(2)\n\t"
"v_mfma_f32_16x16x32_bf16 v[64:67], v[96:99], a[96:99], v[64:67]\n\t"
"v_mfma_f32_16x16x32_bf16 v[68:71], v[96:99], a[100:103], v[68:71]\n\t"
"ds_read_b128 v[96:99], v44 offset:12288\n\t"
"v_mfma_f32_16x16x32_bf16 v[76:79], v[100:103], a[100:103], v[76:79]\n\t"
"v_mfma_f32_16x16x32_bf16 v[72:75], v[100:103], a[96:99], v[72:75]\n\t"
"ds_read_b128 v[100:103], v44 offset:12544\n\t"
"s_waitcnt lgkmcnt(2)\n\t"
"v_mfma_f32_16x16x32_bf16 v[64:67], v[104:107], a[104:107], v[64:67]\n\t"
"s_add_u32 s52, s38, s53\n\t"
"s_add_u32 m0, s39, s53\n\t"
"v_mfma_f32_16x16x32_bf16 v[68:71], v[104:107], a[108:111], v[68:71]\n\t"
"ds_read_b128 v[104:107], v44 offset:14336\n\t"
"v_mfma_f32_16x16x32_bf16 v[76:79], v[108:111], a[108:111], v[76:79]\n\t"
"v_mfma_f32_16x16x32_bf16 v[72:75], v[108:111], a[104:107], v[72:75]\n\t"
"ds_read_b128 v[108:111], v44 offset:14592\n\t"
"s_waitcnt lgkmcnt(2)\n\t"
"v_mfma_f32_16x16x32_bf16 v[64:67], v[96:99], a[112:115], v[64:67]\n\t"
"s_waitcnt vmcnt(12)\n\t"
"s_barrier\n\t"
"v_add_u32 v44, s57, v35\n\t"
"v_mfma_f32_16x16x32_bf16 v[68:71], v[96:99], a[116:119], v[68:71]\n\t"
"ds_read_b128 v[96:99], v44 offset:0\n\t"
"v_mfma_f32_16x16x32_bf16 v[76:79], v[100:103], a[116:119], v[76:79]\n\t"
"buffer_load_dwordx4 v34, %[rs], s52 offen offset:0 sc1 lds\n\t"
"v_mfma_f32_16x16x32_bf16 v[72:75], v[100:103], a[112:115], v[72:75]\n\t"
"ds_read_b128 v[100:103], v44 offset:256\n\t"
"s_waitcnt lgkmcnt(2)\n\t"
"v_mfma_f32_16x16x32_bf16 v[64:67], v[104:107], a[120:123], v[64:67]\n\t"
"v_mfma_f32_16x16x32_bf16 v[68:71], v[104:107], a[124:127], v[68:71]\n\t"
"ds_read_b128 v[104:107], v44 offset:2048\n\t"
"v_mfma_f32_16x16x32_bf16 v[76:79], v[108:111], a[124:127], v[76:79]\n\t"
"v_mfma_f32_16x16x32_bf16 v[72:75], v[108:111], a[120:123], v[72:75]\n\t"
"ds_read_b128 v[108:111], v44 offset:2304\n\t"
"s_waitcnt lgkmcnt(2)\n\t"
"v_mfma_f32_16x16x32_bf16 v[64:67], v[96:99], a[128:131], v[64:67]\n\t"
"buffer_load_dwordx4 v34, %[rs], s52 offen offset:1024 sc1 lds\n\t"
"v_mfma_f32_16x16x32_bf16 v[68:71], v[96:99], a[132:135], v[68:71]\n\t"
"ds_read_b128 v[96:99], v44 offset:4096\n\t"
"v_mfma_f32_16x16x32_bf16 v[76:79], v[100:103], a[132:135], v[76:79]\n\t"
"s_waitcnt vmcnt(4)\n\t"
"buffer_store_dwordx2 v[32:33], v36, %[rs], s41 offen sc1\n\t"
"v_mfma_f32_16x16x32_bf16 v[72:75], v[100:103], a[128:131], v[72:75]\n\t"
"ds_read_b128 v[100:103], v44 offset:4352\n\t"
"s_waitcnt lgkmcnt(2)\n\t"
"v_mfma_f32_16x16x32_bf16 v[64:67], v[104:107], a[136:139], v[64:67]\n\t"
"v_cmp_ne_u64 vcc, v[120:121], v[32:33]\n\t"
"v_mfma_f32_16x16x32_bf16 v[68:71], v[104:107], a[140:143], v[68:71]\n\t"
"ds_read_b128 v[104:107], v44 offset:6144\n\t"
"v_mfma_f32_16x16x32_bf16 v[76:79], v[108:111], a[140:143], v[76:79]\n\t"
"buffer_load_dwordx4 v34, %[rs], s52 offen offset:2048 sc1 lds\n\t"
"v_mfma_f32_16x16x32_bf16 v[72:75], v[108:111], a[136:139], v[72:75]\n\t"
"ds_read_b128 v[108:111], v44 offset:6400\n\t"
"s_waitcnt lgkmcnt(2)\n\t"
"v_mfma_f32_16x16x32_bf16 v[64:67], v[96:99], a[144:147], v[64:67]\n\t"
"s_mov_b32 s53, s58\n\t"
"s_mov_b32 s58, s57\n\t"
"s_add_u32 s57, s57, 16384\n\t"
"v_mfma_f32_16x16x32_bf16 v[68:71], v[96:99], a[148:151], v[68:71]\n\t"
"ds_read_b128 v[96:99], v44 offset:8192\n\t"
"v_mfma_f32_16x16x32_bf16 v[76:79], v[100:103], a[148:151], v[76:79]\n\t"
"s_cmp_eq_u32 s57, 147456\n\t"
"s_cselect_b32 s57, 49152, s57\n\t"
"v_mfma_f32_16x16x32_bf16 v[72:75], v[100:103], a[144:147], v[72:75]\n\t"
"ds_read_b128 v[100:103], v44 offset:8448\n\t"
"s_waitcnt lgkmcnt(2)\n\t"
"v_mfma_f32_16x16x32_bf16 v[64:67], v[104:107], a[152:155], v[64:67]\n\t"
"buffer_load_dwordx4 v34, %[rs], s52 offen offset:3072 sc1 lds\n\t"
"v_mfma_f32_16x16x32_bf16 v[68:71], v[104:107], a[156:159], v[68:71]\n\t"
"ds_read_b128 v[104:107], v44 offset:10240\n\t"
"v_mfma_f32_16x16x32_bf16 v[76:79], v[108:111], a[156:159], v[76:79]\n\t"
"s_cbranch_vccnz L_a2_nl0_%=\n\t"
"buffer_load_dwordx4 v[128:131], v35, %[rs], s42 offen offset:0 sc1\n\t"
"L_a2_nl0_%=:\n\t"
"v_mfma_f32_16x16x32_bf16 v[72:75], v[108:111], a[152:155], v[72:75]\n\t"
"ds_read_b128 v[108:111], v44 offset:10496\n\t"
"s_waitcnt lgkmcnt(2)\n\t"
"v_mfma_f32_16x16x32_bf16 v[64:67], v[96:99], a[160:163], v[64:67]\n\t"
"s_cbranch_vccnz L_a2_nl1_%=\n\t"
"buffer_load_dwordx4 v[132:135], v35, %[rs], s42 offen offset:256 sc1\n\t"
"L_a2_nl1_%=:\n\t"
"v_mfma_f32_16x16x32_bf16 v[68:71], v[96:99], a[164:167], v[68:71]\n\t"
"ds_read_b128 v[96:99], v44 offset:12288\n\t"
"v_mfma_f32_16x16x32_bf16 v[76:79], v[100:103], a[164:167], v[76:79]\n\t"
"s_cbranch_vccnz L_a2_nl2_%=\n\t"
"buffer_load_dwordx4 v[136:139], v35, %[rs], s43 offen offset:0 sc1\n\t"
"L_a2_nl2_%=:\n\t"
"v_mfma_f32_16x16x32_bf16 v[72:75], v[100:103], a[160:163], v[72:75]\n\t"
"ds_read_b128 v[100:103], v44 offset:12544\n\t"
"s_waitcnt lgkmcnt(2)\n\t"
"v_mfma_f32_16x16x32_bf16 v[64:67], v[104:107], a[168:171], v[64:67]\n\t"
"s_add_u32 s52, s38, s53\n\t"
"s_add_u32 m0, s39, s53\n\t"
"v_mfma_f32_16x16x32_bf16 v[68:71], v[104:107], a[172:175], v[68:71]\n\t"
"ds_read_b128 v[104:107], v44 offset:14336\n\t"
"v_mfma_f32_16x16x32_bf16 v[76:79], v[108:111], a[172:175], v[76:79]\n\t"
"s_cbranch_vccnz L_a2_nl3_%=\n\t"
"buffer_load_dwordx4 v[140:143], v35, %[rs], s43 offen offset:256 sc1\n\t"
"L_a2_nl3_%=:\n\t"
"v_mfma_f32_16x16x32_bf16 v[72:75], v[108:111], a[168:171], v[72:75]\n\t"
"ds_read_b128 v[108:111], v44 offset:14592\n\t"
"s_waitcnt lgkmcnt(2)\n\t"
"v_mfma_f32_16x16x32_bf16 v[64:67], v[96:99], a[176:179], v[64:67]\n\t"
"s_waitcnt vmcnt(12)\n\t"
"s_barrier\n\t"
"v_add_u32 v44, s57, v35\n\t"
"v_mfma_f32_16x16x32_bf16 v[68:71], v[96:99], a[180:183], v[68:71]\n\t"
"ds_read_b128 v[96:99], v44 offset:0\n\t"
"v_mfma_f32_16x16x32_bf16 v[76:79], v[100:103], a[180:183], v[76:79]\n\t"
"s_cbranch_vccnz L_a2_nl4_%=\n\t"
"buffer_load_dwordx4 v[144:147], v35, %[rs], s44 offen offset:0 sc1\n\t"
"L_a2_nl4_%=:\n\t"
"v_mfma_f32_16x16x32_bf16 v[72:75], v[100:103], a[176:179], v[72:75]\n\t"
"ds_read_b128 v[100:103], v44 offset:256\n\t"
"s_waitcnt lgkmcnt(2)\n\t"
"v_mfma_f32_16x16x32_bf16 v[64:67], v[104:107], a[184:187], v[64:67]\n\t"
"s_cbranch_vccnz L_a2_nl5_%=\n\t"
"buffer_load_dwordx4 v[148:151], v35, %[rs], s44 offen offset:256 sc1\n\t"
"L_a2_nl5_%=:\n\t"
"v_mfma_f32_16x16x32_bf16 v[68:71], v[104:107], a[188:191], v[68:71]\n\t"
"ds_read_b128 v[104:107], v44 offset:2048\n\t"
"v_mfma_f32_16x16x32_bf16 v[76:79], v[108:111], a[188:191], v[76:79]\n\t"
"s_cbranch_vccnz L_a2_nl6_%=\n\t"
"buffer_load_dwordx4 v[152:155], v35, %[rs], s45 offen offset:0 sc1\n\t"
"L_a2_nl6_%=:\n\t"
"v_mfma_f32_16x16x32_bf16 v[72:75], v[108:111], a[184:187], v[72:75]\n\t"
"ds_read_b128 v[108:111], v44 offset:2304\n\t"
"s_waitcnt lgkmcnt(2)\n\t"
"v_mfma_f32_16x16x32_bf16 v[64:67], v[96:99], a[192:195], v[64:67]\n\t"
"s_cbranch_vccnz L_a2_nl7_%=\n\t"
"buffer_load_dwordx4 v[156:159], v35, %[rs], s45 offen offset:256 sc1\n\t"
"L_a2_nl7_%=:\n\t"
"v_mfma_f32_16x16x32_bf16 v[68:71], v[96:99], a[196:199], v[68:71]\n\t"
"ds_read_b128 v[96:99], v44 offset:4096\n\t"
"v_mfma_f32_16x16x32_bf16 v[76:79], v[100:103], a[196:199], v[76:79]\n\t"
"v_mfma_f32_16x16x32_bf16 v[72:75], v[100:103], a[192:195], v[72:75]\n\t"
"ds_read_b128 v[100:103], v44 offset:4352\n\t"
"s_waitcnt lgkmcnt(2)\n\t"
"v_mfma_f32_16x16x32_bf16 v[64:67], v[104:107], a[200:203], v[64:67]\n\t"
"buffer_load_dwordx2 v[120:121], v37, %[rs], 0 offen sc1\n\t"
"v_mfma_f32_16x16x32_bf16 v[68:71], v[104:107], a[204:207], v[68:71]\n\t"
"ds_read_b128 v[104:107], v44 offset:6144\n\t"
"v_mfma_f32_16x16x32_bf16 v[76:79], v[108:111], a[204:207], v[76:79]\n\t"
"v_mfma_f32_16x16x32_bf16 v[72:75], v[108:111], a[200:203], v[72:75]\n\t"
"ds_read_b128 v[108:111], v44 offset:6400\n\t"
"s_waitcnt lgkmcnt(2)\n\t"
"v_mfma_f32_16x16x32_bf16 v[64:67], v[96:99], a[208:211], v[64:67]\n\t"
"s_mov_b32 s53, s58\n\t"
"s_mov_b32 s58, s57\n\t"
"s_add_u32 s57, s57, 16384\n\t"
"v_mfma_f32_16x16x32_bf16 v[68:71], v[96:99], a[212:215], v[68:71]\n\t"
"ds_read_b128 v[96:99], v44 offset:8192\n\t"
"v_mfma_f32_16x16x32_bf16 v[76:79], v[100:103], a[212:215], v[76:79]\n\t"
"s_cmp_eq_u32 s57, 147456\n\t"
"s_cselect_b32 s57, 49152, s57\n\t"
"v_mfma_f32_16x16x32_bf16 v[72:75], v[100:103], a[208:211], v[72:75]\n\t"
"ds_read_b128 v[100:103], v44 offset:8448\n\t"
"s_waitcnt lgkmcnt(2)\n\t"
"v_mfma_f32_16x16x32_bf16 v[64:67], v[104:107], a[216:219], v[64:67]\n\t"
"v_mfma_f32_16x16x32_bf16 v[68:71], v[104:107], a[220:223], v[68:71]\n\t"
"ds_read_b128 v[104:107], v44 offset:10240\n\t"
"v_mfma_f32_16x16x32_bf16 v[76:79], v[108:111], a[220:223], v[76:79]\n\t"
"v_mfma_f32_16x16x32_bf16 v[72:75], v[108:111], a[216:219], v[72:75]\n\t"
"ds_read_b128 v[108:111], v44 offset:10496\n\t"
"s_waitcnt lgkmcnt(2)\n\t"
"v_mfma_f32_16x16x32_bf16 v[64:67], v[96:99], a[224:227], v[64:67]\n\t"
"v_mfma_f32_16x16x32_bf16 v[68:71], v[96:99], a[228:231], v[68:71]\n\t"
"ds_read_b128 v[96:99], v44 offset:12288\n\t"
"v_mfma_f32_16x16x32_bf16 v[76:79], v[100:103], a[228:231], v[76:79]\n\t"
"v_mfma_f32_16x16x32_bf16 v[72:75], v[100:103], a[224:227], v[72:75]\n\t"
"ds_read_b128 v[100:103], v44 offset:12544\n\t"
"s_waitcnt lgkmcnt(2)\n\t"
"v_mfma_f32_16x16x32_bf16 v[64:67], v[104:107], a[232:235], v[64:67]\n\t"
"s_add_u32 s52, s38, s53\n\t"
"s_add_u32 m0, s39, s53\n\t"
"v_mfma_f32_16x16x32_bf16 v[68:71], v[104:107], a[236:239], v[68:71]\n\t"
"ds_read_b128 v[104:107], v44 offset:14336\n\t"
"v_mfma_f32_16x16x32_bf16 v[76:79], v[108:111], a[236:239], v[76:79]\n\t"
"v_mfma_f32_16x16x32_bf16 v[72:75], v[108:111], a[232:235], v[72:75]\n\t"
"ds_read_b128 v[108:111], v44 offset:14592\n\t"
"s_waitcnt lgkmcnt(2)\n\t"
"v_mfma_f32_16x16x32_bf16 v[64:67], v[96:99], a[240:243], v[64:67]\n\t"
"v_mfma_f32_16x16x32_bf16 v[68:71], v[96:99], a[244:247], v[68:71]\n\t"
"ds_read_b128 v[96:99], v44 offset:0\n\t"
"v_mfma_f32_16x16x32_bf16 v[76:79], v[100:103], a[244:247], v[76:79]\n\t"
"v_mfma_f32_16x16x32_bf16 v[72:75], v[100:103], a[240:243], v[72:75]\n\t"
"ds_read_b128 v[100:103], v44 offset:256\n\t"
"s_waitcnt lgkmcnt(2)\n\t"
"v_mfma_f32_16x16x32_bf16 v[198:201], v[104:107], a[248:251], v[64:67]\n\t"
"v_mfma_f32_16x16x32_bf16 v[202:205], v[104:107], a[252:255], v[68:71]\n\t"
"ds_read_b128 v[104:107], v44 offset:2048\n\t"
"v_mfma_f32_16x16x32_bf16 v[210:213], v[108:111], a[252:255], v[76:79]\n\t"
"v_mfma_f32_16x16x32_bf16 v[206:209], v[108:111], a[248:251], v[72:75]\n\t"
"ds_read_b128 v[108:111], v44 offset:2304\n\t"
"s_waitcnt vmcnt(1) lgkmcnt(0)\n\t"
"s_barrier\n\t"
"s_nop 4\n\t"
"v_cvt_pk_bf16_f32 v112, v198, v199\n\t"
"v_cvt_pk_bf16_f32 v113, v200, v201\n\t"
"v_cvt_pk_bf16_f32 v114, v206, v207\n\t"
"v_cvt_pk_bf16_f32 v115, v208, v209\n\t"
"v_cvt_pk_bf16_f32 v116, v202, v203\n\t"
"v_cvt_pk_bf16_f32 v117, v204, v205\n\t"
"v_cvt_pk_bf16_f32 v118, v210, v211\n\t"
"v_cvt_pk_bf16_f32 v119, v212, v213\n\t"
"buffer_store_dwordx4 v[112:115], v38, %[rs], s37 offen offset:0 sc1\n\t"
"buffer_store_dwordx4 v[116:119], v38, %[rs], s37 offen offset:256 sc1\n\t"
"s_cbranch_vccz L_a2_have_%=\n\t"
"s_waitcnt vmcnt(2)\n\t"
"v_cmp_ne_u64 vcc, v[120:121], v[32:33]\n\t"
"s_nop 4\n\t"
"s_cbranch_vccz L_a2c_ok_%=\n\t"
"s_mov_b32 s52, 0x40000\n\t"
"L_a2c_spin_%=:\n\t"
"buffer_load_dwordx2 v[120:121], v37, %[rs], 0 offen sc1\n\t"
"s_waitcnt vmcnt(0)\n\t"
"v_cmp_ne_u64 vcc, v[120:121], v[32:33]\n\t"
"s_sub_u32 s52, s52, 1\n\t"
"s_cbranch_scc1 L_a2c_ok_%=\n\t"
"s_nop 4\n\t"
"s_cbranch_vccnz L_a2c_spin_%=\n\t"
"L_a2c_ok_%=:\n\t"
"buffer_load_dwordx4 v[128:131], v35, %[rs], s42 offen offset:0 sc1\n\t"
"buffer_load_dwordx4 v[132:135], v35, %[rs], s42 offen offset:256 sc1\n\t"
"buffer_load_dwordx4 v[136:139], v35, %[rs], s43 offen offset:0 sc1\n\t"
"buffer_load_dwordx4 v[140:143], v35, %[rs], s43 offen offset:256 sc1\n\t"
"buffer_load_dwordx4 v[144:147], v35, %[rs], s44 offen offset:0 sc1\n\t"
"buffer_load_dwordx4 v[148:151], v35, %[rs], s44 offen offset:256 sc1\n\t"
"buffer_load_dwordx4 v[152:155], v35, %[rs], s45 offen offset:0 sc1\n\t"
"buffer_load_dwordx4 v[156:159], v35, %[rs], s45 offen offset:256 sc1\n\t"
"s_waitcnt vmcnt(8)\n\t"
"buffer_store_dwordx2 v[32:33], v36, %[rs], s48 offen sc1\n\t"
"v_mov_b32 v36, 0x80000000\n\t"
"s_waitcnt vmcnt(1)\n\t"
"L_a2_have_%=:\n\t"
"v_mfma_f32_16x16x32_bf16 v[80:83], v[246:249], v[128:131], v[80:83]\n\t"
"v_mfma_f32_16x16x32_bf16 v[88:91], v[250:253], v[128:131], v[88:91]\n\t"
"v_mfma_f32_16x16x32_bf16 v[84:87], v[246:249], v[132:135], v[84:87]\n\t"
"v_mfma_f32_16x16x32_bf16 v[92:95], v[250:253], v[132:135], v[92:95]\n\t"
"v_mfma_f32_16x16x32_bf16 v[80:83], v[246:249], v[136:139], v[80:83]\n\t"
"v_mfma_f32_16x16x32_bf16 v[88:91], v[250:253], v[136:139], v[88:91]\n\t"
"v_mfma_f32_16x16x32_bf16 v[84:87], v[246:249], v[140:143], v[84:87]\n\t"
"v_mfma_f32_16x16x32_bf16 v[92:95], v[250:253], v[140:143], v[92:95]\n\t"
"v_mfma_f32_16x16x32_bf16 v[80:83], v[246:249], v[144:147], v[80:83]\n\t"
"v_mfma_f32_16x16x32_bf16 v[88:91], v[250:253], v[144:147], v[88:91]\n\t"
"v_mfma_f32_16x16x32_bf16 v[84:87], v[246:249], v[148:151], v[84:87]\n\t"
"v_mfma_f32_16x16x32_bf16 v[92:95], v[250:253], v[148:151], v[92:95]\n\t"
"v_mfma_f32_16x16x32_bf16 v[80:83], v[246:249], v[152:155], v[80:83]\n\t"
"v_mfma_f32_16x16x32_bf16 v[88:91], v[250:253], v[152:155], v[88:91]\n\t"
"v_mfma_f32_16x16x32_bf16 v[84:87], v[246:249], v[156:159], v[84:87]\n\t"
"v_mfma_f32_16x16x32_bf16 v[92:95], v[250:253], v[156:159], v[92:95]\n\t"
"s_nop 7\n\t"
"v_cvt_pk_bf16_f32 v128, v80, v81\n\t"
"v_cvt_pk_bf16_f32 v129, v82, v83\n\t"
"v_cvt_pk_bf16_f32 v130, v88, v89\n\t"
"v_cvt_pk_bf16_f32 v131, v90, v91\n\t"
"v_cvt_pk_bf16_f32 v132, v84, v85\n\t"
"v_cvt_pk_bf16_f32 v133, v86, v87\n\t"
"v_cvt_pk_bf16_f32 v134, v92, v93\n\t"
"v_cvt_pk_bf16_f32 v135, v94, v95\n\t"
"buffer_store_dwordx4 v[128:131], v35, %[rs], s46 offen offset:0 sc1\n\t"
"buffer_store_dwordx4 v[132:135], v35, %[rs], s46 offen offset:256 sc1\n\t"
"v_pk_add_f32 v[138:139], v[80:81], v[82:83]\n\t"
"v_pk_add_f32 v[140:141], v[84:85], v[86:87]\n\t"
"v_pk_add_f32 v[138:139], v[138:139], v[88:89]\n\t"
"v_pk_add_f32 v[140:141], v[140:141], v[92:93]\n\t"
"v_pk_add_f32 v[138:139], v[138:139], v[90:91]\n\t"
"v_pk_add_f32 v[140:141], v[140:141], v[94:95]\n\t"
"v_add_f32 v138, v138, v139\n\t"
"v_add_f32 v140, v140, v141\n\t"
"v_mov_b32 v142, v138\n\t"
"v_mov_b32 v143, v140\n\t"
"s_nop 0\n\t"
"v_permlane16_swap_b32 v138, v142\n\t"
"v_permlane16_swap_b32 v140, v143\n\t"
"v_add_f32 v138, v138, v142\n\t"
"v_add_f32 v140, v140, v143\n\t"
"v_mov_b32 v142, v138\n\t"
"v_mov_b32 v143, v140\n\t"
"s_nop 0\n\t"
"v_permlane32_swap_b32 v138, v142\n\t"
"v_permlane32_swap_b32 v140, v143\n\t"
"v_add_f32 v138, v138, v142\n\t"
"v_add_f32 v140, v140, v143\n\t"
"v_mul_f32 v138, 0x3d000000, v138\n\t"
"v_mul_f32 v140, 0x3d000000, v140\n\t"
"v_pk_add_f32 v[80:81], v[80:81], v[138:139] op_sel_hi:[1,0] neg_lo:[0,1] neg_hi:[0,1]\n\t"
"v_pk_add_f32 v[84:85], v[84:85], v[140:141] op_sel_hi:[1,0] neg_lo:[0,1] neg_hi:[0,1]\n\t"
"v_pk_add_f32 v[82:83], v[82:83], v[138:139] op_sel_hi:[1,0] neg_lo:[0,1] neg_hi:[0,1]\n\t"
"v_pk_add_f32 v[86:87], v[86:87], v[140:141] op_sel_hi:[1,0] neg_lo:[0,1] neg_hi:[0,1]\n\t"
"v_pk_add_f32 v[88:89], v[88:89], v[138:139] op_sel_hi:[1,0] neg_lo:[0,1] neg_hi:[0,1]\n\t"
"v_pk_add_f32 v[92:93], v[92:93], v[140:141] op_sel_hi:[1,0] neg_lo:[0,1] neg_hi:[0,1]\n\t"
"s_waitcnt vmcnt(2)\n\t"
"buffer_store_dwordx2 v[32:33], v36, %[rs], s48 offen sc1\n\t"
"v_pk_add_f32 v[90:91], v[90:91], v[138:139] op_sel_hi:[1,0] neg_lo:[0,1] neg_hi:[0,1]\n\t"
"v_pk_add_f32 v[94:95], v[94:95], v[140:141] op_sel_hi:[1,0] neg_lo:[0,1] neg_hi:[0,1]\n\t"
"v_pk_mul_f32 v[80:81], v[80:81], v[80:81]\n\t"
"v_pk_mul_f32 v[84:85], v[84:85], v[84:85]\n\t"
"v_pk_fma_f32 v[80:81], v[82:83], v[82:83], v[80:81]\n\t"
"v_pk_fma_f32 v[84:85], v[86:87], v[86:87], v[84:85]\n\t"
"v_pk_fma_f32 v[80:81], v[88:89], v[88:89], v[80:81]\n\t"
"v_pk_fma_f32 v[84:85], v[92:93], v[92:93], v[84:85]\n\t"
"v_pk_fma_f32 v[80:81], v[90:91], v[90:91], v[80:81]\n\t"
"v_pk_fma_f32 v[84:85], v[94:95], v[94:95], v[84:85]\n\t"
"v_add_f32 v139, v80, v81\n\t"
"v_add_f32 v141, v84, v85\n\t"
"v_mov_b32 v142, v139\n\t"
"v_mov_b32 v143, v141\n\t"
"s_nop 0\n\t"
"v_permlane16_swap_b32 v139, v142\n\t"
"v_permlane16_swap_b32 v141, v143\n\t"
"v_add_f32 v139, v139, v142\n\t"
"v_add_f32 v141, v141, v143\n\t"
"v_mov_b32 v142, v139\n\t"
"v_mov_b32 v143, v141\n\t"
"s_nop 0\n\t"
"v_permlane32_swap_b32 v139, v142\n\t"
"v_permlane32_swap_b32 v141, v143\n\t"
"v_add_f32 v139, v139, v142\n\t"
"v_add_f32 v141, v141, v143\n\t"
"s_mov_b32 vcc_lo, 0xffff0000\n\t"
"s_mov_b32 vcc_hi, 0xffff0000\n\t"
"v_cndmask_b32 v136, v138, v140, vcc\n\t"
"v_cndmask_b32 v137, v139, v141, vcc\n\t"
"buffer_store_dwordx2 v[136:137], v40, %[rs], s47 offen sc1\n\t"
"s_waitcnt vmcnt(0)\n\t"
"buffer_store_dwordx2 v[32:33], v41, %[rs], s49 offen sc1\n\t"

                   : "+{s[36:39]}"(a2_S0), "+{s[40:43]}"(a2_S1), "+{s[44:47]}"(a2_S2), "+{s[48:51]}"(a2_S3), "+{v[32:35]}"(a2_V0), "+{v[36:39]}"(a2_V1), "+{v[40:43]}"(a2_V2), "=&{v[198:213]}"(late)
                   : [rs] "s"(rs), "{v[246:253]}"(sel)
                   : "memory", "scc", "vcc", "s52","s53","s54","s55","s56","s57","s58","s59","s60","s61","s62","s63","s64","s65","s66","s67","v44","v45","v64","v65","v66","v67","v68","v69","v70","v71","v72","v73","v74","v75","v76","v77","v78","v79","v80","v81","v82","v83","v84","v85","v86","v87","v88","v89","v90","v91","v92","v93","v94","v95","v96","v97","v98","v99","v100","v101","v102","v103","v104","v105","v106","v107","v108","v109","v110","v111","v112","v113","v114","v115","v116","v117","v118","v119","v120","v121","v122","v123","v124","v125","v126","v127","v128","v129","v130","v131","v132","v133","v134","v135","v136","v137","v138","v139","v140","v141","v142","v143","v144","v145","v146","v147","v148","v149","v150","v151","v152","v153","v154","v155","v156","v157","v158","v159", ACLOB);
      if (w == ks) {
        const unsigned lt_xb = OFF_XA + ly * XU_B + ub + 8192, lt_q0 = PART_O(1) + 8192, lt_q1 = PART_O(2) + 8192, lt_q2 = PART_O(3) + 8192;
        const unsigned lt_xo = OFF_X1 + ly * XU_B + ub + 8192, lt_to = OFF_ST1 + ly * ST1_B + (unsigned)((cb * 5 + 4) * 2048 + pt * 32) * 8u;
        const unsigned lt_vp = q * 512 + r * 16, lt_vb = flp + 128 + (lane & 3) * 8, lt_vt = hh ? OOB : j32 * 8u;
        const unsigned lt_vx = fline(ly, FL_XA, (4 * rb + w) * 2 + half) + (min(lane & 3, 2) + (lane & 12)) * 8;
        v4u lt_S0 = {(unsigned)(lt_xb), (unsigned)(lt_q0), (unsigned)(lt_q1), (unsigned)(lt_q2)}; v4u lt_S1 = {(unsigned)(lt_xo), (unsigned)(lt_to), (unsigned)(lt_to), (unsigned)(lt_to)}; v4u lt_V0 = {ep0, ep1, (unsigned)(lt_vp), (unsigned)(lt_vb)}; v4u lt_V1 = {(unsigned)(lt_vt), (unsigned)(lt_vx), (unsigned)(lt_vx), (unsigned)(lt_vx)};
      asm volatile("" : "+{s[36:39]}"(lt_S0), "+{s[40:43]}"(lt_S1));
      asm volatile("" : "+{v[32:35]}"(lt_V0), "+{v[36:39]}"(lt_V1));
        v2u pl;
        asm volatile("buffer_load_dwordx4 v[214:217], v34, %[rs], s36 offen offset:0 sc1\n\t"
"buffer_load_dwordx4 v[218:221], v34, %[rs], s36 offen offset:256 sc1\n\t"
"buffer_load_dwordx2 v[196:197], v35, %[rs], 0 offen sc1\n\t"
"s_waitcnt vmcnt(0)\n\t"
"v_cmp_ne_u64 vcc, v[196:197], v[32:33]\n\t"
"s_nop 4\n\t"
"s_cbranch_vccz L_lt_ok_%=\n\t"
"s_mov_b32 s44, 0x40000\n\t"
"L_lt_spin_%=:\n\t"
"buffer_load_dwordx2 v[196:197], v35, %[rs], 0 offen sc1\n\t"
"s_waitcnt vmcnt(0)\n\t"
"v_cmp_ne_u64 vcc, v[196:197], v[32:33]\n\t"
"s_sub_u32 s44, s44, 1\n\t"
"s_cbranch_scc1 L_lt_ok_%=\n\t"
"s_nop 4\n\t"
"s_cbranch_vccnz L_lt_spin_%=\n\t"
"L_lt_ok_%=:\n\t"
"buffer_load_dwordx4 v[222:225], v34, %[rs], s37 offen offset:0 sc1\n\t"
"buffer_load_dwordx4 v[226:229], v34, %[rs], s37 offen offset:256 sc1\n\t"
"buffer_load_dwordx4 v[230:233], v34, %[rs], s38 offen offset:0 sc1\n\t"
"buffer_load_dwordx4 v[234:237], v34, %[rs], s38 offen offset:256 sc1\n\t"
"buffer_load_dwordx4 v[238:241], v34, %[rs], s39 offen offset:0 sc1\n\t"
"buffer_load_dwordx4 v[242:245], v34, %[rs], s39 offen offset:256 sc1\n\t"
"v_mfma_f32_16x16x32_bf16 v[198:201], v[246:249], v[214:217], v[198:201]\n\t"
"v_mfma_f32_16x16x32_bf16 v[206:209], v[250:253], v[214:217], v[206:209]\n\t"
"v_mfma_f32_16x16x32_bf16 v[202:205], v[246:249], v[218:221], v[202:205]\n\t"
"v_mfma_f32_16x16x32_bf16 v[210:213], v[250:253], v[218:221], v[210:213]\n\t"
"s_waitcnt vmcnt(5)\n\t"
"v_mfma_f32_16x16x32_bf16 v[198:201], v[246:249], v[222:225], v[198:201]\n\t"
"v_mfma_f32_16x16x32_bf16 v[206:209], v[250:253], v[222:225], v[206:209]\n\t"
"s_waitcnt vmcnt(4)\n\t"
"v_mfma_f32_16x16x32_bf16 v[202:205], v[246:249], v[226:229], v[202:205]\n\t"
"v_mfma_f32_16x16x32_bf16 v[210:213], v[250:253], v[226:229], v[210:213]\n\t"
"s_waitcnt vmcnt(3)\n\t"
"v_mfma_f32_16x16x32_bf16 v[198:201], v[246:249], v[230:233], v[198:201]\n\t"
"v_mfma_f32_16x16x32_bf16 v[206:209], v[250:253], v[230:233], v[206:209]\n\t"
"s_waitcnt vmcnt(2)\n\t"
"v_mfma_f32_16x16x32_bf16 v[202:205], v[246:249], v[234:237], v[202:205]\n\t"
"v_mfma_f32_16x16x32_bf16 v[210:213], v[250:253], v[234:237], v[210:213]\n\t"
"s_waitcnt vmcnt(1)\n\t"
"v_mfma_f32_16x16x32_bf16 v[198:201], v[246:249], v[238:241], v[198:201]\n\t"
"v_mfma_f32_16x16x32_bf16 v[206:209], v[250:253], v[238:241], v[206:209]\n\t"
"s_waitcnt vmcnt(0)\n\t"
"v_mfma_f32_16x16x32_bf16 v[202:205], v[246:249], v[242:245], v[202:205]\n\t"
"v_mfma_f32_16x16x32_bf16 v[210:213], v[250:253], v[242:245], v[210:213]\n\t"
"buffer_load_dwordx2 v[196:197], v37, %[rs], 0 offen sc1\n\t"
"s_nop 6\n\t"
"v_cvt_pk_bf16_f32 v214, v198, v199\n\t"
"v_cvt_pk_bf16_f32 v215, v200, v201\n\t"
"v_cvt_pk_bf16_f32 v216, v206, v207\n\t"
"v_cvt_pk_bf16_f32 v217, v208, v209\n\t"
"v_cvt_pk_bf16_f32 v218, v202, v203\n\t"
"v_cvt_pk_bf16_f32 v219, v204, v205\n\t"
"v_cvt_pk_bf16_f32 v220, v210, v211\n\t"
"v_cvt_pk_bf16_f32 v221, v212, v213\n\t"
"buffer_store_dwordx4 v[214:217], v34, %[rs], s40 offen offset:0 sc1\n\t"
"buffer_store_dwordx4 v[218:221], v34, %[rs], s40 offen offset:256 sc1\n\t"
"v_pk_add_f32 v[224:225], v[198:199], v[200:201]\n\t"
"v_pk_add_f32 v[226:227], v[202:203], v[204:205]\n\t"
"v_pk_add_f32 v[224:225], v[224:225], v[206:207]\n\t"
"v_pk_add_f32 v[226:227], v[226:227], v[210:211]\n\t"
"v_pk_add_f32 v[224:225], v[224:225], v[208:209]\n\t"
"v_pk_add_f32 v[226:227], v[226:227], v[212:213]\n\t"
"v_add_f32 v224, v224, v225\n\t"
"v_add_f32 v226, v226, v227\n\t"
"v_mov_b32 v228, v224\n\t"
"v_mov_b32 v229, v226\n\t"
"s_nop 0\n\t"
"v_permlane16_swap_b32 v224, v228\n\t"
"v_permlane16_swap_b32 v226, v229\n\t"
"v_add_f32 v224, v224, v228\n\t"
"v_add_f32 v226, v226, v229\n\t"
"v_mov_b32 v228, v224\n\t"
"v_mov_b32 v229, v226\n\t"
"s_nop 0\n\t"
"v_permlane32_swap_b32 v224, v228\n\t"
"v_permlane32_swap_b32 v226, v229\n\t"
"v_add_f32 v224, v224, v228\n\t"
"v_add_f32 v226, v226, v229\n\t"
"v_mul_f32 v224, 0x3d000000, v224\n\t"
"v_mul_f32 v226, 0x3d000000, v226\n\t"
"v_pk_add_f32 v[198:199], v[198:199], v[224:225] op_sel_hi:[1,0] neg_lo:[0,1] neg_hi:[0,1]\n\t"
"v_pk_add_f32 v[202:203], v[202:203], v[226:227] op_sel_hi:[1,0] neg_lo:[0,1] neg_hi:[0,1]\n\t"
"v_pk_add_f32 v[200:201], v[200:201], v[224:225] op_sel_hi:[1,0] neg_lo:[0,1] neg_hi:[0,1]\n\t"
"v_pk_add_f32 v[204:205], v[204:205], v[226:227] op_sel_hi:[1,0] neg_lo:[0,1] neg_hi:[0,1]\n\t"
"v_pk_add_f32 v[206:207], v[206:207], v[224:225] op_sel_hi:[1,0] neg_lo:[0,1] neg_hi:[0,1]\n\t"
"v_pk_add_f32 v[210:211], v[210:211], v[226:227] op_sel_hi:[1,0] neg_lo:[0,1] neg_hi:[0,1]\n\t"
"v_pk_add_f32 v[208:209], v[208:209], v[224:225] op_sel_hi:[1,0] neg_lo:[0,1] neg_hi:[0,1]\n\t"
"v_pk_add_f32 v[212:213], v[212:213], v[226:227] op_sel_hi:[1,0] neg_lo:[0,1] neg_hi:[0,1]\n\t"
"v_pk_mul_f32 v[198:199], v[198:199], v[198:199]\n\t"
"v_pk_mul_f32 v[202:203], v[202:203], v[202:203]\n\t"
"v_pk_fma_f32 v[198:199], v[200:201], v[200:201], v[198:199]\n\t"
"v_pk_fma_f32 v[202:203], v[204:205], v[204:205], v[202:203]\n\t"
"v_pk_fma_f32 v[198:199], v[206:207], v[206:207], v[198:199]\n\t"
"v_pk_fma_f32 v[202:203], v[210:211], v[210:211], v[202:203]\n\t"
"v_pk_fma_f32 v[198:199], v[208:209], v[208:209], v[198:199]\n\t"
"v_pk_fma_f32 v[202:203], v[212:213], v[212:213], v[202:203]\n\t"
"v_add_f32 v225, v198, v199\n\t"
"v_add_f32 v227, v202, v203\n\t"
"v_mov_b32 v228, v225\n\t"
"v_mov_b32 v229, v227\n\t"
"s_nop 0\n\t"
"v_permlane16_swap_b32 v225, v228\n\t"
"v_permlane16_swap_b32 v227, v229\n\t"
"v_add_f32 v225, v225, v228\n\t"
"v_add_f32 v227, v227, v229\n\t"
"v_mov_b32 v228, v225\n\t"
"v_mov_b32 v229, v227\n\t"
"s_nop 0\n\t"
"v_permlane32_swap_b32 v225, v228\n\t"
"v_permlane32_swap_b32 v227, v229\n\t"
"v_add_f32 v225, v225, v228\n\t"
"v_add_f32 v227, v227, v229\n\t"
"s_mov_b32 vcc_lo, 0xffff0000\n\t"
"s_mov_b32 vcc_hi, 0xffff0000\n\t"
"v_cndmask_b32 v222, v224, v226, vcc\n\t"
"v_cndmask_b32 v223, v225, v227, vcc\n\t"
"buffer_store_dwordx2 v[222:223], v36, %[rs], s41 offen sc1\n\t"
"s_waitcnt vmcnt(3)\n\t"

                     : "+{s[36:39]}"(lt_S0), "+{s[40:43]}"(lt_S1), "+{v[32:35]}"(lt_V0), "+{v[36:39]}"(lt_V1), "+{v[198:213]}"(late), "=&{v[196:197]}"(pl)
                     : [rs] "s"(rs), "{v[246:253]}"(sel)
                     : "memory", "scc", "vcc", "s44","v214","v215","v216","v217","v218","v219","v220","v221","v222","v223","v224","v225","v226","v227","v228","v229","v230","v231","v232","v233","v234","v235","v236","v237","v238","v239","v240","v241","v242","v243","v244","v245", ACLOB);
        xa_ok = __all(pl.x == ev.x && pl.y == ev.y);
      } else {
        const int j = (w - ks - 1) & 3;
#pragma unroll 1
        for (int i = 0; i < 15; ++i) { const int kb = min(96 + 3 * i + j, 139); dma_sc1(kb * 1024, lane16, rs, a2_sw + kb * 1024); }
      }
    }
    {
      PHASE_VARS();
      const int pt = 4 * rb + w;
      const unsigned b1_sp = OFF_X1 + ly * XU_B + pt * 40960, b1_ss = OFF_ST1 + ly * ST1_B + pt * 256, b1_sm = w * 1024;
      const unsigned b1_s1 = OFF_W1T + c16 * W1T_SL, b1_s2 = OFF_W2T + (4 * half + (c16 & 3)) * W2T_SL;
      const unsigned b1_f1 = fline(ly, FL_XA, pt * 2 + half), b1_go = fline(ly, FL_XB, (4 * rb2 + w) * 2) + (cb * 4 + 3) * 8;
      const unsigned b1_vg = (w == ks && lane < 2) ? lane * 128u : OOB;
      const unsigned b1_ho = OFF_H + ly * H_B + (unsigned)(pt * 216 + 2 * (27 * (c16 >> 2) + 7 * (c16 & 3))) * 512u;
      const unsigned b1_hf = fline(ly, FL_H, (pt * 2 + half) * 2) + (c16 & 7) * 8;
      const unsigned b1_vp = q * 512 + r * 16, b1_vl = lane16, b1_vf = (lane & 3) * 32 + 24;
      const unsigned b1_vs = q * 81920 + r * 8;
      const unsigned b1_v2 = lane < 2 ? lane * 128u : OOB, b1_lc = L_CS1 + q * 16;
      const unsigned b1_v8 = (c16 & 3) != 3 ? (q >> 1) * 512 + r * 16 + (q & 1) * 8 : OOB;
      v4u b1_S0 = {(unsigned)(b1_sp), (unsigned)(b1_ss), (unsigned)(b1_sm), (unsigned)(b1_s1)}; v4u b1_S1 = {(unsigned)(b1_s2), (unsigned)(b1_f1), (unsigned)(b1_ho), (unsigned)(b1_hf)}; v4u b1_S2 = {(unsigned)(b1_go), (unsigned)(b1_go), (unsigned)(b1_go), (unsigned)(b1_go)}; v4u b1_V0 = {ep0, ep1, (unsigned)(b1_vp), (unsigned)(b1_vl)}; v4u b1_V1 = {(unsigned)(b1_vf), (unsigned)(b1_vs), (unsigned)(b1_v2), (unsigned)(b1_lc)}; v4u b1_V2 = {(unsigned)(b1_v8), (unsigned)(b1_vg), (unsigned)(b1_vg), (unsigned)(b1_vg)};
      asm volatile("" : "+{s[36:39]}"(b1_S0), "+{s[40:43]}"(b1_S1), "+{s[44:47]}"(b1_S2));
      asm volatile("" : "+{v[32:35]}"(b1_V0), "+{v[36:39]}"(b1_V1), "+{v[40:43]}"(b1_V2));
      if (ly == 0) {
        float c4[4];
#pragma unroll
        for (int i = 0; i < 4; ++i) c4[i] = __uint_as_float(__builtin_amdgcn_raw_buffer_load_b32(rs, tid < 112 ? tid * 4u : OOB, OFF_CS + (i * 1792 + c16 * 112) * 4, SC1));
        if (tid < 112) *(LAS float*)(L + L_CS1 + tid * 4) = (c4[0] + c4[1]) + (c4[2] + c4[3]);
      }
      if (!xa_ok) poll_spin((min(lane & 3, 2) + (lane & 12)) * 8, rs, b1_f1, v2u{ep0, ep1});
      asm volatile("s_add_u32 s48, s36, 0\n\t"
"buffer_load_dwordx4 v[140:143], v34, %[rs], s48 offen sc1\n\t"
"buffer_load_dwordx4 v[144:147], v34, %[rs], s48 offen offset:256 sc1\n\t"
"s_add_u32 s48, s36, 2048\n\t"
"buffer_load_dwordx4 v[148:151], v34, %[rs], s48 offen sc1\n\t"
"buffer_load_dwordx4 v[152:155], v34, %[rs], s48 offen offset:256 sc1\n\t"
"s_add_u32 s48, s36, 4096\n\t"
"buffer_load_dwordx4 v[156:159], v34, %[rs], s48 offen sc1\n\t"
"buffer_load_dwordx4 v[160:163], v34, %[rs], s48 offen offset:256 sc1\n\t"
"s_add_u32 s48, s36, 10240\n\t"
"buffer_load_dwordx4 v[164:167], v34, %[rs], s48 offen sc1\n\t"
"buffer_load_dwordx4 v[168:171], v34, %[rs], s48 offen offset:256 sc1\n\t"
"s_add_u32 s48, s36, 12288\n\t"
"buffer_load_dwordx4 v[172:175], v34, %[rs], s48 offen sc1\n\t"
"buffer_load_dwordx4 v[176:179], v34, %[rs], s48 offen offset:256 sc1\n\t"
"v_add_u32 v44, 61440, v35\n\t"
"v_add_u32 v45, 122880, v35\n\t"
"v_add_u32 v46, s38, v35\n\t"
"s_add_u32 s50, s41, 16384\n\t"
"ds_read_b128 v[120:123], v35 offset:0\n\t"
"ds_read_b128 v[124:127], v35 offset:1024\n\t"
"ds_read_b128 v[128:131], v35 offset:2048\n\t"
"ds_read_b128 v[132:135], v35 offset:3072\n\t"
"ds_read_b128 v[136:139], v35 offset:4096\n\t"
"v_mov_b32 v64, 0\n\t"
"v_mov_b32 v65, 0\n\t"
"v_mov_b32 v66, 0\n\t"
"v_mov_b32 v67, 0\n\t"
"v_mov_b32 v68, 0\n\t"
"v_mov_b32 v69, 0\n\t"
"v_mov_b32 v70, 0\n\t"
"v_mov_b32 v71, 0\n\t"
"v_mov_b32 v72, 0\n\t"
"v_mov_b32 v73, 0\n\t"
"v_mov_b32 v74, 0\n\t"
"v_mov_b32 v75, 0\n\t"
"v_mov_b32 v76, 0\n\t"
"v_mov_b32 v77, 0\n\t"
"v_mov_b32 v78, 0\n\t"
"v_mov_b32 v79, 0\n\t"
"v_mov_b32 v80, 0\n\t"
"v_mov_b32 v81, 0\n\t"
"v_mov_b32 v82, 0\n\t"
"v_mov_b32 v83, 0\n\t"
"v_mov_b32 v84, 0\n\t"
"v_mov_b32 v85, 0\n\t"
"v_mov_b32 v86, 0\n\t"
"v_mov_b32 v87, 0\n\t"
"v_mov_b32 v88, 0\n\t"
"v_mov_b32 v89, 0\n\t"
"v_mov_b32 v90, 0\n\t"
"v_mov_b32 v91, 0\n\t"
"v_mov_b32 v92, 0\n\t"
"v_mov_b32 v93, 0\n\t"
"v_mov_b32 v94, 0\n\t"
"v_mov_b32 v95, 0\n\t"
"v_mov_b32 v96, 0\n\t"
"v_mov_b32 v97, 0\n\t"
"v_mov_b32 v98, 0\n\t"
"v_mov_b32 v99, 0\n\t"
"v_mov_b32 v100, 0\n\t"
"v_mov_b32 v101, 0\n\t"
"v_mov_b32 v102, 0\n\t"
"v_mov_b32 v103, 0\n\t"
"v_mov_b32 v104, 0\n\t"
"v_mov_b32 v105, 0\n\t"
"v_mov_b32 v106, 0\n\t"
"v_mov_b32 v107, 0\n\t"
"v_mov_b32 v108, 0\n\t"
"v_mov_b32 v109, 0\n\t"
"v_mov_b32 v110, 0\n\t"
"v_mov_b32 v111, 0\n\t"
"v_mov_b32 v112, 0\n\t"
"v_mov_b32 v113, 0\n\t"
"v_mov_b32 v114, 0\n\t"
"v_mov_b32 v115, 0\n\t"
"v_mov_b32 v116, 0\n\t"
"v_mov_b32 v117, 0\n\t"
"v_mov_b32 v118, 0\n\t"
"v_mov_b32 v119, 0\n\t"
"s_waitcnt vmcnt(8) lgkmcnt(3)\n\t"
"v_mfma_f32_16x16x32_bf16 v[64:67], v[120:123], v[140:143], v[64:67]\n\t"
"s_add_u32 s48, s36, 14336\n\t"
"v_mfma_f32_16x16x32_bf16 v[68:71], v[120:123], v[144:147], v[68:71]\n\t"
"ds_read_b128 v[120:123], v35 offset:5120\n\t"
"v_mfma_f32_16x16x32_bf16 v[72:75], v[124:127], v[140:143], v[72:75]\n\t"
"buffer_store_dwordx2 v[32:33], v41, %[rs], s44 offen sc1\n\t"
"v_mfma_f32_16x16x32_bf16 v[76:79], v[124:127], v[144:147], v[76:79]\n\t"
"ds_read_b128 v[124:127], v35 offset:6144\n\t"
"s_waitcnt lgkmcnt(3)\n\t"
"v_mfma_f32_16x16x32_bf16 v[80:83], v[128:131], v[140:143], v[80:83]\n\t"
"buffer_load_dwordx4 v[180:183], v34, %[rs], s48 offen sc1\n\t"
"v_mfma_f32_16x16x32_bf16 v[84:87], v[128:131], v[144:147], v[84:87]\n\t"
"ds_read_b128 v[128:131], v35 offset:7168\n\t"
"v_mfma_f32_16x16x32_bf16 v[88:91], v[132:135], v[140:143], v[88:91]\n\t"
"buffer_load_dwordx4 v[184:187], v34, %[rs], s48 offen offset:256 sc1\n\t"
"v_mfma_f32_16x16x32_bf16 v[92:95], v[132:135], v[144:147], v[92:95]\n\t"
"ds_read_b128 v[132:135], v35 offset:8192\n\t"
"s_waitcnt lgkmcnt(3)\n\t"
"v_mfma_f32_16x16x32_bf16 v[96:99], v[136:139], v[140:143], v[96:99]\n\t"
"s_add_u32 s48, s36, 20480\n\t"
"v_mfma_f32_16x16x32_bf16 v[100:103], v[136:139], v[144:147], v[100:103]\n\t"
"ds_read_b128 v[136:139], v35 offset:9216\n\t"
"v_mfma_f32_16x16x32_bf16 v[104:107], v[120:123], v[140:143], v[104:107]\n\t"
"buffer_load_dwordx4 v[188:191], v34, %[rs], s48 offen sc1\n\t"
"v_mfma_f32_16x16x32_bf16 v[108:111], v[120:123], v[144:147], v[108:111]\n\t"
"ds_read_b128 v[120:123], v35 offset:10240\n\t"
"s_waitcnt lgkmcnt(3)\n\t"
"v_mfma_f32_16x16x32_bf16 v[112:115], v[124:127], v[140:143], v[112:115]\n\t"
"buffer_load_dwordx4 v[192:195], v34, %[rs], s48 offen offset:256 sc1\n\t"
"v_mfma_f32_16x16x32_bf16 v[116:119], v[124:127], v[144:147], v[116:119]\n\t"
"ds_read_b128 v[124:127], v35 offset:11264\n\t"
"s_waitcnt vmcnt(11)\n\t"
"v_mfma_f32_16x16x32_bf16 v[64:67], v[128:131], v[148:151], v[64:67]\n\t"
"s_add_u32 s48, s36, 22528\n\t"
"v_mfma_f32_16x16x32_bf16 v[68:71], v[128:131], v[152:155], v[68:71]\n\t"
"ds_read_b128 v[128:131], v35 offset:12288\n\t"
"s_waitcnt lgkmcnt(3)\n\t"
"v_mfma_f32_16x16x32_bf16 v[72:75], v[132:135], v[148:151], v[72:75]\n\t"
"buffer_load_dwordx4 v[140:143], v34, %[rs], s48 offen sc1\n\t"
"v_mfma_f32_16x16x32_bf16 v[76:79], v[132:135], v[152:155], v[76:79]\n\t"
"ds_read_b128 v[132:135], v35 offset:13312\n\t"
"v_mfma_f32_16x16x32_bf16 v[80:83], v[136:139], v[148:151], v[80:83]\n\t"
"buffer_load_dwordx4 v[144:147], v34, %[rs], s48 offen offset:256 sc1\n\t"
"v_mfma_f32_16x16x32_bf16 v[84:87], v[136:139], v[152:155], v[84:87]\n\t"
"ds_read_b128 v[136:139], v35 offset:14336\n\t"
"s_waitcnt lgkmcnt(3)\n\t"
"v_mfma_f32_16x16x32_bf16 v[88:91], v[120:123], v[148:151], v[88:91]\n\t"
"v_mfma_f32_16x16x32_bf16 v[92:95], v[120:123], v[152:155], v[92:95]\n\t"
"ds_read_b128 v[120:123], v35 offset:15360\n\t"
"v_mfma_f32_16x16x32_bf16 v[96:99], v[124:127], v[148:151], v[96:99]\n\t"
"v_mfma_f32_16x16x32_bf16 v[100:103], v[124:127], v[152:155], v[100:103]\n\t"
"ds_read_b128 v[124:127], v35 offset:16384\n\t"
"s_waitcnt lgkmcnt(3)\n\t"
"v_mfma_f32_16x16x32_bf16 v[104:107], v[128:131], v[148:151], v[104:107]\n\t"
"v_mfma_f32_16x16x32_bf16 v[108:111], v[128:131], v[152:155], v[108:111]\n\t"
"ds_read_b128 v[128:131], v35 offset:17408\n\t"
"s_add_u32 s48, s36, 24576\n\t"
"v_mfma_f32_16x16x32_bf16 v[112:115], v[132:135], v[148:151], v[112:115]\n\t"
"v_mfma_f32_16x16x32_bf16 v[116:119], v[132:135], v[152:155], v[116:119]\n\t"
"ds_read_b128 v[132:135], v35 offset:18432\n\t"
"s_waitcnt vmcnt(11) lgkmcnt(3)\n\t"
"v_mfma_f32_16x16x32_bf16 v[64:67], v[136:139], v[156:159], v[64:67]\n\t"
"buffer_load_dwordx4 v[148:151], v34, %[rs], s48 offen sc1\n\t"
"v_mfma_f32_16x16x32_bf16 v[68:71], v[136:139], v[160:163], v[68:71]\n\t"
"ds_read_b128 v[136:139], v35 offset:19456\n\t"
"v_mfma_f32_16x16x32_bf16 v[72:75], v[120:123], v[156:159], v[72:75]\n\t"
"buffer_load_dwordx4 v[152:155], v34, %[rs], s48 offen offset:256 sc1\n\t"
"v_mfma_f32_16x16x32_bf16 v[76:79], v[120:123], v[160:163], v[76:79]\n\t"
"ds_read_b128 v[120:123], v35 offset:20480\n\t"
"s_waitcnt lgkmcnt(3)\n\t"
"v_mfma_f32_16x16x32_bf16 v[80:83], v[124:127], v[156:159], v[80:83]\n\t"
"v_mfma_f32_16x16x32_bf16 v[84:87], v[124:127], v[160:163], v[84:87]\n\t"
"ds_read_b128 v[124:127], v35 offset:21504\n\t"
"v_mfma_f32_16x16x32_bf16 v[88:91], v[128:131], v[156:159], v[88:91]\n\t"
"v_mfma_f32_16x16x32_bf16 v[92:95], v[128:131], v[160:163], v[92:95]\n\t"
"ds_read_b128 v[128:131], v35 offset:22528\n\t"
"s_waitcnt lgkmcnt(3)\n\t"
"v_mfma_f32_16x16x32_bf16 v[96:99], v[132:135], v[156:159], v[96:99]\n\t"
"v_mfma_f32_16x16x32_bf16 v[100:103], v[132:135], v[160:163], v[100:103]\n\t"
"ds_read_b128 v[132:135], v35 offset:23552\n\t"
"v_mfma_f32_16x16x32_bf16 v[104:107], v[136:139], v[156:159], v[104:107]\n\t"
"v_mfma_f32_16x16x32_bf16 v[108:111], v[136:139], v[160:163], v[108:111]\n\t"
"ds_read_b128 v[136:139], v35 offset:24576\n\t"
"s_waitcnt lgkmcnt(3)\n\t"
"v_mfma_f32_16x16x32_bf16 v[112:115], v[120:123], v[156:159], v[112:115]\n\t"
"s_add_u32 s48, s36, 30720\n\t"
"v_mfma_f32_16x16x32_bf16 v[116:119], v[120:123], v[160:163], v[116:119]\n\t"
"ds_read_b128 v[120:123], v35 offset:25600\n\t"
"s_waitcnt vmcnt(11)\n\t"
"v_mfma_f32_16x16x32_bf16 v[64:67], v[124:127], v[164:167], v[64:67]\n\t"
"buffer_load_dwordx2 v[196:197], v36, %[rs], s41 offen sc1\n\t"
"v_mfma_f32_16x16x32_bf16 v[68:71], v[124:127], v[168:171], v[68:71]\n\t"
"ds_read_b128 v[124:127], v35 offset:26624\n\t"
"s_waitcnt lgkmcnt(3)\n\t"
"v_mfma_f32_16x16x32_bf16 v[72:75], v[128:131], v[164:167], v[72:75]\n\t"
"buffer_load_dwordx4 v[156:159], v34, %[rs], s48 offen sc1\n\t"
"v_mfma_f32_16x16x32_bf16 v[76:79], v[128:131], v[168:171], v[76:79]\n\t"
"ds_read_b128 v[128:131], v35 offset:27648\n\t"
"v_mfma_f32_16x16x32_bf16 v[80:83], v[132:135], v[164:167], v[80:83]\n\t"
"buffer_load_dwordx4 v[160:163], v34, %[rs], s48 offen offset:256 sc1\n\t"
"v_mfma_f32_16x16x32_bf16 v[84:87], v[132:135], v[168:171], v[84:87]\n\t"
"ds_read_b128 v[132:135], v35 offset:28672\n\t"
"s_waitcnt lgkmcnt(3)\n\t"
"v_mfma_f32_16x16x32_bf16 v[88:91], v[136:139], v[164:167], v[88:91]\n\t"
"v_mfma_f32_16x16x32_bf16 v[92:95], v[136:139], v[168:171], v[92:95]\n\t"
"ds_read_b128 v[136:139], v35 offset:29696\n\t"
"v_mfma_f32_16x16x32_bf16 v[96:99], v[120:123], v[164:167], v[96:99]\n\t"
"v_mfma_f32_16x16x32_bf16 v[100:103], v[120:123], v[168:171], v[100:103]\n\t"
"ds_read_b128 v[120:123], v35 offset:30720\n\t"
"s_waitcnt lgkmcnt(3)\n\t"
"v_mfma_f32_16x16x32_bf16 v[104:107], v[124:127], v[164:167], v[104:107]\n\t"
"v_mfma_f32_16x16x32_bf16 v[108:111], v[124:127], v[168:171], v[108:111]\n\t"
"ds_read_b128 v[124:127], v35 offset:31744\n\t"
"s_add_u32 s48, s36, 32768\n\t"
"v_mfma_f32_16x16x32_bf16 v[112:115], v[128:131], v[164:167], v[112:115]\n\t"
"v_mfma_f32_16x16x32_bf16 v[116:119], v[128:131], v[168:171], v[116:119]\n\t"
"ds_read_b128 v[128:131], v35 offset:32768\n\t"
"s_waitcnt vmcnt(12) lgkmcnt(3)\n\t"
"v_mfma_f32_16x16x32_bf16 v[64:67], v[132:135], v[172:175], v[64:67]\n\t"
"buffer_load_dwordx4 v[164:167], v34, %[rs], s48 offen sc1\n\t"
"v_mfma_f32_16x16x32_bf16 v[68:71], v[132:135], v[176:179], v[68:71]\n\t"
"ds_read_b128 v[132:135], v35 offset:33792\n\t"
"v_mfma_f32_16x16x32_bf16 v[72:75], v[136:139], v[172:175], v[72:75]\n\t"
"buffer_load_dwordx4 v[168:171], v34, %[rs], s48 offen offset:256 sc1\n\t"
"v_mfma_f32_16x16x32_bf16 v[76:79], v[136:139], v[176:179], v[76:79]\n\t"
"ds_read_b128 v[136:139], v35 offset:34816\n\t"
"s_waitcnt lgkmcnt(3)\n\t"
"v_mfma_f32_16x16x32_bf16 v[80:83], v[120:123], v[172:175], v[80:83]\n\t"
"v_mfma_f32_16x16x32_bf16 v[84:87], v[120:123], v[176:179], v[84:87]\n\t"
"ds_read_b128 v[120:123], v35 offset:35840\n\t"
"v_mfma_f32_16x16x32_bf16 v[88:91], v[124:127], v[172:175], v[88:91]\n\t"
"v_mfma_f32_16x16x32_bf16 v[92:95], v[124:127], v[176:179], v[92:95]\n\t"
"ds_read_b128 v[124:127], v35 offset:36864\n\t"
"s_waitcnt lgkmcnt(3)\n\t"
"v_mfma_f32_16x16x32_bf16 v[96:99], v[128:131], v[172:175], v[96:99]\n\t"
"v_mfma_f32_16x16x32_bf16 v[100:103], v[128:131], v[176:179], v[100:103]\n\t"
"ds_read_b128 v[128:131], v35 offset:37888\n\t"
"v_mfma_f32_16x16x32_bf16 v[104:107], v[132:135], v[172:175], v[104:107]\n\t"
"v_mfma_f32_16x16x32_bf16 v[108:111], v[132:135], v[176:179], v[108:111]\n\t"
"ds_read_b128 v[132:135], v35 offset:38912\n\t"
"s_waitcnt lgkmcnt(3)\n\t"
"v_mfma_f32_16x16x32_bf16 v[112:115], v[136:139], v[172:175], v[112:115]\n\t"
"s_add_u32 s48, s36, 34816\n\t"
"v_mfma_f32_16x16x32_bf16 v[116:119], v[136:139], v[176:179], v[116:119]\n\t"
"ds_read_b128 v[136:139], v35 offset:39936\n\t"
"s_waitcnt vmcnt(11)\n\t"
"v_mfma_f32_16x16x32_bf16 v[64:67], v[120:123], v[180:183], v[64:67]\n\t"
"buffer_load_dwordx4 v[172:175], v34, %[rs], s48 offen sc1\n\t"
"v_mfma_f32_16x16x32_bf16 v[68:71], v[120:123], v[184:187], v[68:71]\n\t"
"ds_read_b128 v[120:123], v35 offset:40960\n\t"
"s_waitcnt lgkmcnt(3)\n\t"
"v_mfma_f32_16x16x32_bf16 v[72:75], v[124:127], v[180:183], v[72:75]\n\t"
"buffer_load_dwordx4 v[176:179], v34, %[rs], s48 offen offset:256 sc1\n\t"
"v_mfma_f32_16x16x32_bf16 v[76:79], v[124:127], v[184:187], v[76:79]\n\t"
"ds_read_b128 v[124:127], v35 offset:41984\n\t"
"s_add_u32 s48, s37, 0\n\t"
"v_mfma_f32_16x16x32_bf16 v[80:83], v[128:131], v[180:183], v[80:83]\n\t"
"buffer_load_dword v198, v37, %[rs], s48 offen sc1\n\t"
"v_mfma_f32_16x16x32_bf16 v[84:87], v[128:131], v[184:187], v[84:87]\n\t"
"ds_read_b128 v[128:131], v35 offset:43008\n\t"
"s_waitcnt lgkmcnt(3)\n\t"
"v_mfma_f32_16x16x32_bf16 v[88:91], v[132:135], v[180:183], v[88:91]\n\t"
"buffer_load_dword v200, v37, %[rs], s48 offen offset:128 sc1\n\t"
"v_mfma_f32_16x16x32_bf16 v[92:95], v[132:135], v[184:187], v[92:95]\n\t"
"ds_read_b128 v[132:135], v35 offset:44032\n\t"
"s_add_u32 s48, s37, 16384\n\t"
"v_mfma_f32_16x16x32_bf16 v[96:99], v[136:139], v[180:183], v[96:99]\n\t"
"buffer_load_dword v202, v37, %[rs], s48 offen sc1\n\t"
"v_mfma_f32_16x16x32_bf16 v[100:103], v[136:139], v[184:187], v[100:103]\n\t"
"ds_read_b128 v[136:139], v35 offset:45056\n\t"
"s_waitcnt lgkmcnt(3)\n\t"
"v_mfma_f32_16x16x32_bf16 v[104:107], v[120:123], v[180:183], v[104:107]\n\t"
"buffer_load_dword v204, v37, %[rs], s48 offen offset:128 sc1\n\t"
"v_mfma_f32_16x16x32_bf16 v[108:111], v[120:123], v[184:187], v[108:111]\n\t"
"ds_read_b128 v[120:123], v35 offset:46080\n\t"
"s_add_u32 s48, s37, 32768\n\t"
"v_mfma_f32_16x16x32_bf16 v[112:115], v[124:127], v[180:183], v[112:115]\n\t"
"buffer_load_dword v206, v37, %[rs], s48 offen sc1\n\t"
"v_mfma_f32_16x16x32_bf16 v[116:119], v[124:127], v[184:187], v[116:119]\n\t"
"ds_read_b128 v[124:127], v35 offset:47104\n\t"
"s_waitcnt vmcnt(16) lgkmcnt(3)\n\t"
"v_mfma_f32_16x16x32_bf16 v[64:67], v[128:131], v[188:191], v[64:67]\n\t"
"buffer_load_dword v208, v37, %[rs], s48 offen offset:128 sc1\n\t"
"v_mfma_f32_16x16x32_bf16 v[68:71], v[128:131], v[192:195], v[68:71]\n\t"
"ds_read_b128 v[128:131], v35 offset:48128\n\t"
"v_mfma_f32_16x16x32_bf16 v[72:75], v[132:135], v[188:191], v[72:75]\n\t"
"s_waitcnt vmcnt(12)\n\t"
"v_cmp_ne_u64 vcc, v[196:197], v[32:33]\n\t"
"s_nop 4\n\t"
"s_cbranch_vccz L_b1c1_ok_%=\n\t"
"s_mov_b32 s48, 0x40000\n\t"
"L_b1c1_spin_%=:\n\t"
"buffer_load_dwordx2 v[196:197], v36, %[rs], s41 offen sc1\n\t"
"s_waitcnt vmcnt(0)\n\t"
"v_cmp_ne_u64 vcc, v[196:197], v[32:33]\n\t"
"s_sub_u32 s48, s48, 1\n\t"
"s_cbranch_scc1 L_b1c1_ok_%=\n\t"
"s_nop 4\n\t"
"s_cbranch_vccnz L_b1c1_spin_%=\n\t"
"L_b1c1_ok_%=:\n\t"
"v_mfma_f32_16x16x32_bf16 v[76:79], v[132:135], v[192:195], v[76:79]\n\t"
"ds_read_b128 v[132:135], v35 offset:49152\n\t"
"s_waitcnt lgkmcnt(3)\n\t"
"v_mfma_f32_16x16x32_bf16 v[80:83], v[136:139], v[188:191], v[80:83]\n\t"
"s_add_u32 s48, s36, 6144\n\t"
"v_mfma_f32_16x16x32_bf16 v[84:87], v[136:139], v[192:195], v[84:87]\n\t"
"ds_read_b128 v[136:139], v35 offset:50176\n\t"
"v_mfma_f32_16x16x32_bf16 v[88:91], v[120:123], v[188:191], v[88:91]\n\t"
"buffer_load_dwordx4 v[180:183], v34, %[rs], s48 offen sc1\n\t"
"v_mfma_f32_16x16x32_bf16 v[92:95], v[120:123], v[192:195], v[92:95]\n\t"
"ds_read_b128 v[120:123], v35 offset:51200\n\t"
"s_waitcnt lgkmcnt(3)\n\t"
"v_mfma_f32_16x16x32_bf16 v[96:99], v[124:127], v[188:191], v[96:99]\n\t"
"buffer_load_dwordx4 v[184:187], v34, %[rs], s48 offen offset:256 sc1\n\t"
"v_mfma_f32_16x16x32_bf16 v[100:103], v[124:127], v[192:195], v[100:103]\n\t"
"ds_read_b128 v[124:127], v35 offset:52224\n\t"
"v_mfma_f32_16x16x32_bf16 v[104:107], v[128:131], v[188:191], v[104:107]\n\t"
"v_mfma_f32_16x16x32_bf16 v[108:111], v[128:131], v[192:195], v[108:111]\n\t"
"ds_read_b128 v[128:131], v35 offset:53248\n\t"
"s_waitcnt lgkmcnt(3)\n\t"
"v_mfma_f32_16x16x32_bf16 v[112:115], v[132:135], v[188:191], v[112:115]\n\t"
"s_add_u32 s48, s36, 16384\n\t"
"v_mfma_f32_16x16x32_bf16 v[116:119], v[132:135], v[192:195], v[116:119]\n\t"
"ds_read_b128 v[132:135], v35 offset:54272\n\t"
"v_mfma_f32_16x16x32_bf16 v[64:67], v[136:139], v[140:143], v[64:67]\n\t"
"buffer_load_dwordx4 v[188:191], v34, %[rs], s48 offen sc1\n\t"
"v_mfma_f32_16x16x32_bf16 v[68:71], v[136:139], v[144:147], v[68:71]\n\t"
"ds_read_b128 v[136:139], v35 offset:55296\n\t"
"s_waitcnt lgkmcnt(3)\n\t"
"v_mfma_f32_16x16x32_bf16 v[72:75], v[120:123], v[140:143], v[72:75]\n\t"
"buffer_load_dwordx4 v[192:195], v34, %[rs], s48 offen offset:256 sc1\n\t"
"v_mfma_f32_16x16x32_bf16 v[76:79], v[120:123], v[144:147], v[76:79]\n\t"
"ds_read_b128 v[120:123], v35 offset:56320\n\t"
"v_mfma_f32_16x16x32_bf16 v[80:83], v[124:127], v[140:143], v[80:83]\n\t"
"v_mfma_f32_16x16x32_bf16 v[84:87], v[124:127], v[144:147], v[84:87]\n\t"
"ds_read_b128 v[124:127], v35 offset:57344\n\t"
"s_waitcnt lgkmcnt(3)\n\t"
"v_mfma_f32_16x16x32_bf16 v[88:91], v[128:131], v[140:143], v[88:91]\n\t"
"v_mfma_f32_16x16x32_bf16 v[92:95], v[128:131], v[144:147], v[92:95]\n\t"
"ds_read_b128 v[128:131], v35 offset:58368\n\t"
"v_mfma_f32_16x16x32_bf16 v[96:99], v[132:135], v[140:143], v[96:99]\n\t"
"buffer_load_dwordx2 v[196:197], v36, %[rs], s50 offen sc1\n\t"
"v_mfma_f32_16x16x32_bf16 v[100:103], v[132:135], v[144:147], v[100:103]\n\t"
"ds_read_b128 v[132:135], v35 offset:59392\n\t"
"s_waitcnt lgkmcnt(3)\n\t"
"v_mfma_f32_16x16x32_bf16 v[104:107], v[136:139], v[140:143], v[104:107]\n\t"
"v_mfma_f32_16x16x32_bf16 v[108:111], v[136:139], v[144:147], v[108:111]\n\t"
"ds_read_b128 v[136:139], v35 offset:60416\n\t"
"s_add_u32 s48, s36, 26624\n\t"
"v_mfma_f32_16x16x32_bf16 v[112:115], v[120:123], v[140:143], v[112:115]\n\t"
"v_mfma_f32_16x16x32_bf16 v[116:119], v[120:123], v[144:147], v[116:119]\n\t"
"ds_read_b128 v[120:123], v44 offset:0\n\t"
"s_waitcnt lgkmcnt(3)\n\t"
"v_mfma_f32_16x16x32_bf16 v[64:67], v[124:127], v[148:151], v[64:67]\n\t"
"s_barrier\n\t"
"v_mfma_f32_16x16x32_bf16 v[68:71], v[124:127], v[152:155], v[68:71]\n\t"
"ds_read_b128 v[124:127], v44 offset:1024\n\t"
"v_mfma_f32_16x16x32_bf16 v[72:75], v[128:131], v[148:151], v[72:75]\n\t"
"buffer_load_dwordx4 v[140:143], v34, %[rs], s48 offen sc1\n\t"
"v_mfma_f32_16x16x32_bf16 v[76:79], v[128:131], v[152:155], v[76:79]\n\t"
"ds_read_b128 v[128:131], v44 offset:2048\n\t"
"s_waitcnt lgkmcnt(3)\n\t"
"v_mfma_f32_16x16x32_bf16 v[80:83], v[132:135], v[148:151], v[80:83]\n\t"
"buffer_load_dwordx4 v[144:147], v34, %[rs], s48 offen offset:256 sc1\n\t"
"v_mfma_f32_16x16x32_bf16 v[84:87], v[132:135], v[152:155], v[84:87]\n\t"
"ds_read_b128 v[132:135], v44 offset:3072\n\t"
"v_mfma_f32_16x16x32_bf16 v[88:91], v[136:139], v[148:151], v[88:91]\n\t"
"s_add_u32 s49, s40, 0\n\t"
"s_add_u32 m0, s38, 0\n\t"
"v_mfma_f32_16x16x32_bf16 v[92:95], v[136:139], v[152:155], v[92:95]\n\t"
"ds_read_b128 v[136:139], v44 offset:4096\n\t"
"s_waitcnt lgkmcnt(3)\n\t"
"v_mfma_f32_16x16x32_bf16 v[96:99], v[120:123], v[148:151], v[96:99]\n\t"
"buffer_load_dwordx4 v46, %[rs], s49 offen sc1 lds\n\t"
"v_mfma_f32_16x16x32_bf16 v[100:103], v[120:123], v[152:155], v[100:103]\n\t"
"ds_read_b128 v[120:123], v44 offset:5120\n\t"
"v_mfma_f32_16x16x32_bf16 v[104:107], v[124:127], v[148:151], v[104:107]\n\t"
"s_add_u32 s49, s40, 4096\n\t"
"s_add_u32 m0, s38, 4096\n\t"
"v_mfma_f32_16x16x32_bf16 v[108:111], v[124:127], v[152:155], v[108:111]\n\t"
"ds_read_b128 v[124:127], v44 offset:6144\n\t"
"s_waitcnt lgkmcnt(3)\n\t"
"v_mfma_f32_16x16x32_bf16 v[112:115], v[128:131], v[148:151], v[112:115]\n\t"
"s_add_u32 s48, s36, 36864\n\t"
"v_mfma_f32_16x16x32_bf16 v[116:119], v[128:131], v[152:155], v[116:119]\n\t"
"ds_read_b128 v[128:131], v44 offset:7168\n\t"
"s_waitcnt vmcnt(18)\n\t"
"v_mfma_f32_16x16x32_bf16 v[64:67], v[132:135], v[156:159], v[64:67]\n\t"
"buffer_load_dwordx4 v[148:151], v34, %[rs], s48 offen sc1\n\t"
"v_mfma_f32_16x16x32_bf16 v[68:71], v[132:135], v[160:163], v[68:71]\n\t"
"ds_read_b128 v[132:135], v44 offset:8192\n\t"
"s_waitcnt lgkmcnt(3)\n\t"
"v_mfma_f32_16x16x32_bf16 v[72:75], v[136:139], v[156:159], v[72:75]\n\t"
"buffer_load_dwordx4 v[152:155], v34, %[rs], s48 offen offset:256 sc1\n\t"
"v_mfma_f32_16x16x32_bf16 v[76:79], v[136:139], v[160:163], v[76:79]\n\t"
"ds_read_b128 v[136:139], v44 offset:9216\n\t"
"s_add_u32 s48, s37, 49152\n\t"
"v_mfma_f32_16x16x32_bf16 v[80:83], v[120:123], v[156:159], v[80:83]\n\t"
"buffer_load_dword v210, v37, %[rs], s48 offen sc1\n\t"
"v_mfma_f32_16x16x32_bf16 v[84:87], v[120:123], v[160:163], v[84:87]\n\t"
"ds_read_b128 v[120:123], v44 offset:10240\n\t"
"s_waitcnt lgkmcnt(3)\n\t"
"v_mfma_f32_16x16x32_bf16 v[88:91], v[124:127], v[156:159], v[88:91]\n\t"
"buffer_load_dword v212, v37, %[rs], s48 offen offset:128 sc1\n\t"
"v_mfma_f32_16x16x32_bf16 v[92:95], v[124:127], v[160:163], v[92:95]\n\t"
"ds_read_b128 v[124:127], v44 offset:11264\n\t"
"v_mfma_f32_16x16x32_bf16 v[96:99], v[128:131], v[156:159], v[96:99]\n\t"
"buffer_load_dwordx4 v46, %[rs], s49 offen sc1 lds\n\t"
"v_mfma_f32_16x16x32_bf16 v[100:103], v[128:131], v[160:163], v[100:103]\n\t"
"ds_read_b128 v[128:131], v44 offset:12288\n\t"
"s_waitcnt lgkmcnt(3)\n\t"
"v_mfma_f32_16x16x32_bf16 v[104:107], v[132:135], v[156:159], v[104:107]\n\t"
"s_add_u32 s49, s40, 8192\n\t"
"s_add_u32 m0, s38, 8192\n\t"
"v_mfma_f32_16x16x32_bf16 v[108:111], v[132:135], v[160:163], v[108:111]\n\t"
"ds_read_b128 v[132:135], v44 offset:13312\n\t"
"v_mfma_f32_16x16x32_bf16 v[112:115], v[136:139], v[156:159], v[112:115]\n\t"
"buffer_load_dwordx4 v46, %[rs], s49 offen sc1 lds\n\t"
"v_mfma_f32_16x16x32_bf16 v[116:119], v[136:139], v[160:163], v[116:119]\n\t"
"ds_read_b128 v[136:139], v44 offset:14336\n\t"
"s_waitcnt vmcnt(22) lgkmcnt(3)\n\t"
"v_mfma_f32_16x16x32_bf16 v[64:67], v[120:123], v[164:167], v[64:67]\n\t"
"s_waitcnt vmcnt(9)\n\t"
"v_cmp_ne_u64 vcc, v[196:197], v[32:33]\n\t"
"s_nop 4\n\t"
"s_cbranch_vccz L_b1c2_ok_%=\n\t"
"s_mov_b32 s48, 0x40000\n\t"
"L_b1c2_spin_%=:\n\t"
"buffer_load_dwordx2 v[196:197], v36, %[rs], s50 offen sc1\n\t"
"s_waitcnt vmcnt(0)\n\t"
"v_cmp_ne_u64 vcc, v[196:197], v[32:33]\n\t"
"s_sub_u32 s48, s48, 1\n\t"
"s_cbranch_scc1 L_b1c2_ok_%=\n\t"
"s_nop 4\n\t"
"s_cbranch_vccnz L_b1c2_spin_%=\n\t"
"L_b1c2_ok_%=:\n\t"
"v_mfma_f32_16x16x32_bf16 v[68:71], v[120:123], v[168:171], v[68:71]\n\t"
"ds_read_b128 v[120:123], v44 offset:15360\n\t"
"s_add_u32 s48, s36, 8192\n\t"
"v_mfma_f32_16x16x32_bf16 v[72:75], v[124:127], v[164:167], v[72:75]\n\t"
"buffer_load_dwordx4 v[156:159], v34, %[rs], s48 offen sc1\n\t"
"v_mfma_f32_16x16x32_bf16 v[76:79], v[124:127], v[168:171], v[76:79]\n\t"
"ds_read_b128 v[124:127], v44 offset:16384\n\t"
"s_waitcnt lgkmcnt(3)\n\t"
"v_mfma_f32_16x16x32_bf16 v[80:83], v[128:131], v[164:167], v[80:83]\n\t"
"buffer_load_dwordx4 v[160:163], v34, %[rs], s48 offen offset:256 sc1\n\t"
"v_mfma_f32_16x16x32_bf16 v[84:87], v[128:131], v[168:171], v[84:87]\n\t"
"ds_read_b128 v[128:131], v44 offset:17408\n\t"
"v_mfma_f32_16x16x32_bf16 v[88:91], v[132:135], v[164:167], v[88:91]\n\t"
"s_add_u32 s49, s40, 12288\n\t"
"s_add_u32 m0, s38, 12288\n\t"
"v_mfma_f32_16x16x32_bf16 v[92:95], v[132:135], v[168:171], v[92:95]\n\t"
"ds_read_b128 v[132:135], v44 offset:18432\n\t"
"s_waitcnt lgkmcnt(3)\n\t"
"v_mfma_f32_16x16x32_bf16 v[96:99], v[136:139], v[164:167], v[96:99]\n\t"
"buffer_load_dwordx4 v46, %[rs], s49 offen sc1 lds\n\t"
"v_mfma_f32_16x16x32_bf16 v[100:103], v[136:139], v[168:171], v[100:103]\n\t"
"ds_read_b128 v[136:139], v44 offset:19456\n\t"
"v_mfma_f32_16x16x32_bf16 v[104:107], v[120:123], v[164:167], v[104:107]\n\t"
"s_add_u32 s49, s40, 16384\n\t"
"s_add_u32 m0, s38, 16384\n\t"
"v_mfma_f32_16x16x32_bf16 v[108:111], v[120:123], v[168:171], v[108:111]\n\t"
"ds_read_b128 v[120:123], v44 offset:20480\n\t"
"s_waitcnt lgkmcnt(3)\n\t"
"v_mfma_f32_16x16x32_bf16 v[112:115], v[124:127], v[164:167], v[112:115]\n\t"
"s_add_u32 s48, s36, 18432\n\t"
"v_mfma_f32_16x16x32_bf16 v[116:119], v[124:127], v[168:171], v[116:119]\n\t"
"ds_read_b128 v[124:127], v44 offset:21504\n\t"
"v_mfma_f32_16x16x32_bf16 v[64:67], v[128:131], v[172:175], v[64:67]\n\t"
"buffer_load_dwordx4 v[164:167], v34, %[rs], s48 offen sc1\n\t"
"v_mfma_f32_16x16x32_bf16 v[68:71], v[128:131], v[176:179], v[68:71]\n\t"
"ds_read_b128 v[128:131], v44 offset:22528\n\t"
"s_waitcnt lgkmcnt(3)\n\t"
"v_mfma_f32_16x16x32_bf16 v[72:75], v[132:135], v[172:175], v[72:75]\n\t"
"buffer_load_dwordx4 v[168:171], v34, %[rs], s48 offen offset:256 sc1\n\t"
"v_mfma_f32_16x16x32_bf16 v[76:79], v[132:135], v[176:179], v[76:79]\n\t"
"ds_read_b128 v[132:135], v44 offset:23552\n\t"
"v_mfma_f32_16x16x32_bf16 v[80:83], v[136:139], v[172:175], v[80:83]\n\t"
"buffer_load_dwordx4 v46, %[rs], s49 offen sc1 lds\n\t"
"v_mfma_f32_16x16x32_bf16 v[84:87], v[136:139], v[176:179], v[84:87]\n\t"
"ds_read_b128 v[136:139], v44 offset:24576\n\t"
"s_waitcnt lgkmcnt(3)\n\t"
"v_mfma_f32_16x16x32_bf16 v[88:91], v[120:123], v[172:175], v[88:91]\n\t"
"s_add_u32 s49, s40, 20480\n\t"
"s_add_u32 m0, s38, 20480\n\t"
"v_mfma_f32_16x16x32_bf16 v[92:95], v[120:123], v[176:179], v[92:95]\n\t"
"ds_read_b128 v[120:123], v44 offset:25600\n\t"
"v_mfma_f32_16x16x32_bf16 v[96:99], v[124:127], v[172:175], v[96:99]\n\t"
"buffer_load_dwordx4 v46, %[rs], s49 offen sc1 lds\n\t"
"v_mfma_f32_16x16x32_bf16 v[100:103], v[124:127], v[176:179], v[100:103]\n\t"
"ds_read_b128 v[124:127], v44 offset:26624\n\t"
"s_waitcnt lgkmcnt(3)\n\t"
"v_mfma_f32_16x16x32_bf16 v[104:107], v[128:131], v[172:175], v[104:107]\n\t"
"s_add_u32 s49, s40, 24576\n\t"
"s_add_u32 m0, s38, 24576\n\t"
"v_mfma_f32_16x16x32_bf16 v[108:111], v[128:131], v[176:179], v[108:111]\n\t"
"ds_read_b128 v[128:131], v44 offset:27648\n\t"
"s_add_u32 s48, s36, 28672\n\t"
"v_mfma_f32_16x16x32_bf16 v[112:115], v[132:135], v[172:175], v[112:115]\n\t"
"buffer_load_dwordx4 v46, %[rs], s49 offen sc1 lds\n\t"
"v_mfma_f32_16x16x32_bf16 v[116:119], v[132:135], v[176:179], v[116:119]\n\t"
"ds_read_b128 v[132:135], v44 offset:28672\n\t"
"s_waitcnt lgkmcnt(3)\n\t"
"v_mfma_f32_16x16x32_bf16 v[64:67], v[136:139], v[180:183], v[64:67]\n\t"
"buffer_load_dwordx4 v[172:175], v34, %[rs], s48 offen sc1\n\t"
"v_mfma_f32_16x16x32_bf16 v[68:71], v[136:139], v[184:187], v[68:71]\n\t"
"ds_read_b128 v[136:139], v44 offset:29696\n\t"
"v_mfma_f32_16x16x32_bf16 v[72:75], v[120:123], v[180:183], v[72:75]\n\t"
"buffer_load_dwordx4 v[176:179], v34, %[rs], s48 offen offset:256 sc1\n\t"
"v_mfma_f32_16x16x32_bf16 v[76:79], v[120:123], v[184:187], v[76:79]\n\t"
"ds_read_b128 v[120:123], v44 offset:30720\n\t"
"s_waitcnt lgkmcnt(3)\n\t"
"v_mfma_f32_16x16x32_bf16 v[80:83], v[124:127], v[180:183], v[80:83]\n\t"
"s_add_u32 s49, s40, 28672\n\t"
"s_add_u32 m0, s38, 28672\n\t"
"v_mfma_f32_16x16x32_bf16 v[84:87], v[124:127], v[184:187], v[84:87]\n\t"
"ds_read_b128 v[124:127], v44 offset:31744\n\t"
"v_mfma_f32_16x16x32_bf16 v[88:91], v[128:131], v[180:183], v[88:91]\n\t"
"buffer_load_dwordx4 v46, %[rs], s49 offen sc1 lds\n\t"
"v_mfma_f32_16x16x32_bf16 v[92:95], v[128:131], v[184:187], v[92:95]\n\t"
"ds_read_b128 v[128:131], v44 offset:32768\n\t"
"s_waitcnt lgkmcnt(3)\n\t"
"v_mfma_f32_16x16x32_bf16 v[96:99], v[132:135], v[180:183], v[96:99]\n\t"
"s_add_u32 s49, s40, 32768\n\t"
"s_add_u32 m0, s38, 32768\n\t"
"v_mfma_f32_16x16x32_bf16 v[100:103], v[132:135], v[184:187], v[100:103]\n\t"
"ds_read_b128 v[132:135], v44 offset:33792\n\t"
"v_mfma_f32_16x16x32_bf16 v[104:107], v[136:139], v[180:183], v[104:107]\n\t"
"buffer_load_dwordx4 v46, %[rs], s49 offen sc1 lds\n\t"
"v_mfma_f32_16x16x32_bf16 v[108:111], v[136:139], v[184:187], v[108:111]\n\t"
"ds_read_b128 v[136:139], v44 offset:34816\n\t"
"s_waitcnt lgkmcnt(3)\n\t"
"v_mfma_f32_16x16x32_bf16 v[112:115], v[120:123], v[180:183], v[112:115]\n\t"
"s_add_u32 s48, s36, 38912\n\t"
"s_add_u32 s49, s40, 36864\n\t"
"s_add_u32 m0, s38, 36864\n\t"
"v_mfma_f32_16x16x32_bf16 v[116:119], v[120:123], v[184:187], v[116:119]\n\t"
"ds_read_b128 v[120:123], v44 offset:35840\n\t"
"v_mfma_f32_16x16x32_bf16 v[64:67], v[124:127], v[188:191], v[64:67]\n\t"
"buffer_load_dwordx4 v[180:183], v34, %[rs], s48 offen sc1\n\t"
"v_mfma_f32_16x16x32_bf16 v[68:71], v[124:127], v[192:195], v[68:71]\n\t"
"ds_read_b128 v[124:127], v44 offset:36864\n\t"
"s_waitcnt lgkmcnt(3)\n\t"
"v_mfma_f32_16x16x32_bf16 v[72:75], v[128:131], v[188:191], v[72:75]\n\t"
"buffer_load_dwordx4 v[184:187], v34, %[rs], s48 offen offset:256 sc1\n\t"
"v_mfma_f32_16x16x32_bf16 v[76:79], v[128:131], v[192:195], v[76:79]\n\t"
"ds_read_b128 v[128:131], v44 offset:37888\n\t"
"s_add_u32 s48, s37, 65536\n\t"
"v_mfma_f32_16x16x32_bf16 v[80:83], v[132:135], v[188:191], v[80:83]\n\t"
"buffer_load_dword v214, v37, %[rs], s48 offen sc1\n\t"
"v_mfma_f32_16x16x32_bf16 v[84:87], v[132:135], v[192:195], v[84:87]\n\t"
"ds_read_b128 v[132:135], v44 offset:38912\n\t"
"s_waitcnt lgkmcnt(3)\n\t"
"v_mfma_f32_16x16x32_bf16 v[88:91], v[136:139], v[188:191], v[88:91]\n\t"
"buffer_load_dword v216, v37, %[rs], s48 offen offset:128 sc1\n\t"
"v_mfma_f32_16x16x32_bf16 v[92:95], v[136:139], v[192:195], v[92:95]\n\t"
"ds_read_b128 v[136:139], v44 offset:39936\n\t"
"v_mfma_f32_16x16x32_bf16 v[96:99], v[120:123], v[188:191], v[96:99]\n\t"
"buffer_load_dwordx4 v46, %[rs], s49 offen sc1 lds\n\t"
"v_mfma_f32_16x16x32_bf16 v[100:103], v[120:123], v[192:195], v[100:103]\n\t"
"ds_read_b128 v[120:123], v44 offset:40960\n\t"
"s_waitcnt lgkmcnt(3)\n\t"
"v_mfma_f32_16x16x32_bf16 v[104:107], v[124:127], v[188:191], v[104:107]\n\t"
"s_add_u32 s49, s40, 40960\n\t"
"s_add_u32 m0, s38, 40960\n\t"
"ds_read_b128 v[218:221], v39 offset:0\n\t"
"v_mfma_f32_16x16x32_bf16 v[108:111], v[124:127], v[192:195], v[108:111]\n\t"
"ds_read_b128 v[124:127], v44 offset:41984\n\t"
"v_mfma_f32_16x16x32_bf16 v[112:115], v[128:131], v[188:191], v[112:115]\n\t"
"buffer_load_dwordx4 v46, %[rs], s49 offen sc1 lds\n\t"
"v_mfma_f32_16x16x32_bf16 v[116:119], v[128:131], v[192:195], v[116:119]\n\t"
"ds_read_b128 v[128:131], v44 offset:43008\n\t"
"s_waitcnt vmcnt(25) lgkmcnt(4)\n\t"
"v_mfma_f32_16x16x32_bf16 v[64:67], v[132:135], v[140:143], v[64:67]\n\t"
"s_barrier\n\t"
"s_add_u32 s49, s40, 45056\n\t"
"s_add_u32 m0, s38, 45056\n\t"
"v_mfma_f32_16x16x32_bf16 v[68:71], v[132:135], v[144:147], v[68:71]\n\t"
"ds_read_b128 v[132:135], v44 offset:44032\n\t"
"v_mfma_f32_16x16x32_bf16 v[72:75], v[136:139], v[140:143], v[72:75]\n\t"
"buffer_load_dwordx4 v46, %[rs], s49 offen sc1 lds\n\t"
"v_mfma_f32_16x16x32_bf16 v[76:79], v[136:139], v[144:147], v[76:79]\n\t"
"ds_read_b128 v[136:139], v44 offset:45056\n\t"
"s_waitcnt lgkmcnt(3)\n\t"
"v_mfma_f32_16x16x32_bf16 v[80:83], v[120:123], v[140:143], v[80:83]\n\t"
"s_add_u32 s49, s40, 49152\n\t"
"s_add_u32 m0, s38, 49152\n\t"
"ds_read_b128 v[222:225], v39 offset:64\n\t"
"v_mfma_f32_16x16x32_bf16 v[84:87], v[120:123], v[144:147], v[84:87]\n\t"
"ds_read_b128 v[120:123], v44 offset:46080\n\t"
"v_mfma_f32_16x16x32_bf16 v[88:91], v[124:127], v[140:143], v[88:91]\n\t"
"buffer_load_dwordx4 v46, %[rs], s49 offen sc1 lds\n\t"
"v_mfma_f32_16x16x32_bf16 v[92:95], v[124:127], v[144:147], v[92:95]\n\t"
"ds_read_b128 v[124:127], v44 offset:47104\n\t"
"s_waitcnt lgkmcnt(4)\n\t"
"v_mfma_f32_16x16x32_bf16 v[96:99], v[128:131], v[140:143], v[96:99]\n\t"
"s_add_u32 s49, s40, 53248\n\t"
"s_add_u32 m0, s38, 53248\n\t"
"ds_read_b128 v[226:229], v39 offset:128\n\t"
"v_mfma_f32_16x16x32_bf16 v[100:103], v[128:131], v[144:147], v[100:103]\n\t"
"ds_read_b128 v[128:131], v44 offset:48128\n\t"
"v_mfma_f32_16x16x32_bf16 v[104:107], v[132:135], v[140:143], v[104:107]\n\t"
"buffer_load_dwordx4 v46, %[rs], s49 offen sc1 lds\n\t"
"v_mfma_f32_16x16x32_bf16 v[108:111], v[132:135], v[144:147], v[108:111]\n\t"
"ds_read_b128 v[132:135], v44 offset:49152\n\t"
"s_waitcnt lgkmcnt(4)\n\t"
"v_mfma_f32_16x16x32_bf16 v[112:115], v[136:139], v[140:143], v[112:115]\n\t"
"s_add_u32 s49, s40, 57344\n\t"
"s_add_u32 m0, s38, 57344\n\t"
"ds_read_b128 v[230:233], v39 offset:192\n\t"
"v_mfma_f32_16x16x32_bf16 v[116:119], v[136:139], v[144:147], v[116:119]\n\t"
"ds_read_b128 v[136:139], v44 offset:50176\n\t"
"s_waitcnt vmcnt(25)\n\t"
"v_mfma_f32_16x16x32_bf16 v[64:67], v[120:123], v[148:151], v[64:67]\n\t"
"buffer_load_dwordx4 v46, %[rs], s49 offen sc1 lds\n\t"
"s_add_u32 s49, s40, 61440\n\t"
"s_add_u32 m0, s38, 61440\n\t"
"ds_read_b128 v[234:237], v39 offset:256\n\t"
"v_mfma_f32_16x16x32_bf16 v[68:71], v[120:123], v[152:155], v[68:71]\n\t"
"ds_read_b128 v[120:123], v44 offset:51200\n\t"
"s_waitcnt lgkmcnt(5)\n\t"
"v_mfma_f32_16x16x32_bf16 v[72:75], v[124:127], v[148:151], v[72:75]\n\t"
"buffer_load_dwordx4 v46, %[rs], s49 offen sc1 lds\n\t"
"s_add_u32 s49, s40, 65536\n\t"
"s_add_u32 m0, s38, 65536\n\t"
"ds_read_b128 v[238:241], v39 offset:320\n\t"
"v_mfma_f32_16x16x32_bf16 v[76:79], v[124:127], v[152:155], v[76:79]\n\t"
"ds_read_b128 v[124:127], v44 offset:52224\n\t"
"v_mfma_f32_16x16x32_bf16 v[80:83], v[128:131], v[148:151], v[80:83]\n\t"
"buffer_load_dwordx4 v46, %[rs], s49 offen sc1 lds\n\t"
"s_add_u32 s49, s40, 69632\n\t"
"s_add_u32 m0, s38, 69632\n\t"
"ds_read_b128 v[242:245], v39 offset:384\n\t"
"v_mfma_f32_16x16x32_bf16 v[84:87], v[128:131], v[152:155], v[84:87]\n\t"
"ds_read_b128 v[128:131], v44 offset:53248\n\t"
"s_waitcnt lgkmcnt(6)\n\t"
"v_mfma_f32_16x16x32_bf16 v[88:91], v[132:135], v[148:151], v[88:91]\n\t"
"buffer_load_dwordx4 v46, %[rs], s49 offen sc1 lds\n\t"
"s_add_u32 s49, s40, 73728\n\t"
"s_add_u32 m0, s38, 73728\n\t"
"v_mfma_f32_16x16x32_bf16 v[92:95], v[132:135], v[152:155], v[92:95]\n\t"
"ds_read_b128 v[132:135], v44 offset:54272\n\t"
"v_mfma_f32_16x16x32_bf16 v[96:99], v[136:139], v[148:151], v[96:99]\n\t"
"buffer_load_dwordx4 v46, %[rs], s49 offen sc1 lds\n\t"
"s_add_u32 s49, s40, 77824\n\t"
"s_add_u32 m0, s38, 77824\n\t"
"v_mfma_f32_16x16x32_bf16 v[100:103], v[136:139], v[152:155], v[100:103]\n\t"
"ds_read_b128 v[136:139], v44 offset:60416\n\t"
"s_waitcnt lgkmcnt(4)\n\t"
"v_mfma_f32_16x16x32_bf16 v[104:107], v[120:123], v[148:151], v[104:107]\n\t"
"buffer_load_dwordx4 v46, %[rs], s49 offen sc1 lds\n\t"
"s_waitcnt vmcnt(11)\n\t"
"v_add_f32 v250, v198, v202\n\t"
"v_add_f32 v252, v206, v210\n\t"
"v_mfma_f32_16x16x32_bf16 v[108:111], v[120:123], v[152:155], v[108:111]\n\t"
"ds_read_b128 v[120:123], v45 offset:0\n\t"
"v_mfma_f32_16x16x32_bf16 v[112:115], v[124:127], v[148:151], v[112:115]\n\t"
"s_add_u32 s49, s40, 81920\n\t"
"s_add_u32 m0, s38, 81920\n\t"
"v_add_f32 v250, v250, v214\n\t"
"v_add_f32 v250, v250, v252\n\t"
"v_mfma_f32_16x16x32_bf16 v[116:119], v[124:127], v[152:155], v[116:119]\n\t"
"ds_read_b128 v[124:127], v45 offset:6144\n\t"
"s_waitcnt lgkmcnt(3)\n\t"
"v_mfma_f32_16x16x32_bf16 v[64:67], v[128:131], v[156:159], v[64:67]\n\t"
"buffer_load_dwordx4 v46, %[rs], s49 offen sc1 lds\n\t"
"v_mov_b32 v252, v250\n\t"
"s_nop 1\n\t"
"v_mfma_f32_16x16x32_bf16 v[68:71], v[128:131], v[160:163], v[68:71]\n\t"
"ds_read_b128 v[128:131], v45 offset:7168\n\t"
"v_mfma_f32_16x16x32_bf16 v[72:75], v[132:135], v[156:159], v[72:75]\n\t"
"s_add_u32 s49, s40, 86016\n\t"
"s_add_u32 m0, s38, 86016\n\t"
"v_permlane16_swap_b32 v250, v252\n\t"
"v_add_f32 v250, v250, v252\n\t"
"v_mfma_f32_16x16x32_bf16 v[76:79], v[132:135], v[160:163], v[76:79]\n\t"
"ds_read_b128 v[132:135], v45 offset:13312\n\t"
"s_waitcnt lgkmcnt(3)\n\t"
"v_mfma_f32_16x16x32_bf16 v[64:67], v[136:139], v[164:167], v[64:67]\n\t"
"buffer_load_dwordx4 v46, %[rs], s49 offen sc1 lds\n\t"
"v_mov_b32 v252, v250\n\t"
"s_nop 1\n\t"
"v_mfma_f32_16x16x32_bf16 v[68:71], v[136:139], v[168:171], v[68:71]\n\t"
"ds_read_b128 v[136:139], v45 offset:14336\n\t"
"v_mfma_f32_16x16x32_bf16 v[72:75], v[120:123], v[164:167], v[72:75]\n\t"
"s_add_u32 s49, s40, 90112\n\t"
"s_add_u32 m0, s38, 90112\n\t"
"v_permlane32_swap_b32 v250, v252\n\t"
"v_add_f32 v250, v250, v252\n\t"
"v_mfma_f32_16x16x32_bf16 v[76:79], v[120:123], v[168:171], v[76:79]\n\t"
"ds_read_b128 v[120:123], v44 offset:55296\n\t"
"s_waitcnt lgkmcnt(3)\n\t"
"v_mfma_f32_16x16x32_bf16 v[64:67], v[124:127], v[172:175], v[64:67]\n\t"
"buffer_load_dwordx4 v46, %[rs], s49 offen sc1 lds\n\t"
"v_mul_f32 v250, 0xbd4ccccd, v250\n\t"
"v_mfma_f32_16x16x32_bf16 v[68:71], v[124:127], v[176:179], v[68:71]\n\t"
"ds_read_b128 v[124:127], v44 offset:56320\n\t"
"v_mfma_f32_16x16x32_bf16 v[72:75], v[128:131], v[172:175], v[72:75]\n\t"
"s_add_u32 s49, s40, 94208\n\t"
"s_add_u32 m0, s38, 94208\n\t"
"v_add_f32 v251, v200, v204\n\t"
"v_add_f32 v252, v208, v212\n\t"
"v_mfma_f32_16x16x32_bf16 v[76:79], v[128:131], v[176:179], v[76:79]\n\t"
"ds_read_b128 v[128:131], v45 offset:1024\n\t"
"s_waitcnt lgkmcnt(3)\n\t"
"v_mfma_f32_16x16x32_bf16 v[64:67], v[132:135], v[180:183], v[64:67]\n\t"
"buffer_load_dwordx4 v46, %[rs], s49 offen sc1 lds\n\t"
"v_add_f32 v251, v251, v216\n\t"
"v_add_f32 v251, v251, v252\n\t"
"v_mfma_f32_16x16x32_bf16 v[68:71], v[132:135], v[184:187], v[68:71]\n\t"
"ds_read_b128 v[132:135], v45 offset:2048\n\t"
"v_mfma_f32_16x16x32_bf16 v[72:75], v[136:139], v[180:183], v[72:75]\n\t"
"v_mov_b32 v252, v251\n\t"
"s_nop 1\n\t"
"v_permlane16_swap_b32 v251, v252\n\t"
"v_add_f32 v251, v251, v252\n\t"
"v_mfma_f32_16x16x32_bf16 v[76:79], v[136:139], v[184:187], v[76:79]\n\t"
"ds_read_b128 v[136:139], v45 offset:8192\n\t"
"s_waitcnt lgkmcnt(3)\n\t"
"v_mfma_f32_16x16x32_bf16 v[80:83], v[120:123], v[156:159], v[80:83]\n\t"
"v_mov_b32 v252, v251\n\t"
"s_nop 1\n\t"
"v_permlane32_swap_b32 v251, v252\n\t"
"v_add_f32 v251, v251, v252\n\t"
"v_mul_f32 v251, 0xbd4ccccd, v251\n\t"
"v_mfma_f32_16x16x32_bf16 v[84:87], v[120:123], v[160:163], v[84:87]\n\t"
"ds_read_b128 v[120:123], v45 offset:9216\n\t"
"v_mfma_f32_16x16x32_bf16 v[88:91], v[124:127], v[156:159], v[88:91]\n\t"
"v_mfma_f32_16x16x32_bf16 v[92:95], v[124:127], v[160:163], v[92:95]\n\t"
"ds_read_b128 v[124:127], v45 offset:15360\n\t"
"s_waitcnt lgkmcnt(3)\n\t"
"v_mfma_f32_16x16x32_bf16 v[80:83], v[128:131], v[164:167], v[80:83]\n\t"
"v_fma_f32 v64, v218, v250, v64\n\t"
"v_fma_f32 v72, v222, v250, v72\n\t"
"v_fma_f32 v65, v219, v250, v65\n\t"
"v_fma_f32 v73, v223, v250, v73\n\t"
"v_mfma_f32_16x16x32_bf16 v[84:87], v[128:131], v[168:171], v[84:87]\n\t"
"ds_read_b128 v[128:131], v45 offset:16384\n\t"
"v_mfma_f32_16x16x32_bf16 v[88:91], v[132:135], v[164:167], v[88:91]\n\t"
"v_fma_f32 v66, v220, v250, v66\n\t"
"v_fma_f32 v74, v224, v250, v74\n\t"
"v_fma_f32 v67, v221, v250, v67\n\t"
"v_fma_f32 v75, v225, v250, v75\n\t"
"v_cvt_pk_bf16_f32 v64, v64, v65\n\t"
"v_cvt_pk_bf16_f32 v65, v66, v67\n\t"
"v_mfma_f32_16x16x32_bf16 v[92:95], v[132:135], v[168:171], v[92:95]\n\t"
"ds_read_b128 v[132:135], v44 offset:57344\n\t"
"s_waitcnt lgkmcnt(3)\n\t"
"v_mfma_f32_16x16x32_bf16 v[80:83], v[136:139], v[172:175], v[80:83]\n\t"
"v_cvt_pk_bf16_f32 v66, v72, v73\n\t"
"v_cvt_pk_bf16_f32 v67, v74, v75\n\t"
"v_pk_max_i16 v64, v64, 0\n\t"
"v_pk_max_i16 v65, v65, 0\n\t"
"v_pk_max_i16 v66, v66, 0\n\t"
"v_pk_max_i16 v67, v67, 0\n\t"
"v_mfma_f32_16x16x32_bf16 v[84:87], v[136:139], v[176:179], v[84:87]\n\t"
"ds_read_b128 v[136:139], v44 offset:58368\n\t"
"v_mfma_f32_16x16x32_bf16 v[88:91], v[120:123], v[172:175], v[88:91]\n\t"
"buffer_store_dwordx4 v[64:67], v34, %[rs], s42 offen offset:0 sc1\n\t"
"v_fma_f32 v68, v218, v251, v68\n\t"
"v_fma_f32 v76, v222, v251, v76\n\t"
"v_fma_f32 v69, v219, v251, v69\n\t"
"v_fma_f32 v77, v223, v251, v77\n\t"
"v_mfma_f32_16x16x32_bf16 v[92:95], v[120:123], v[176:179], v[92:95]\n\t"
"ds_read_b128 v[120:123], v45 offset:3072\n\t"
"s_waitcnt lgkmcnt(3)\n\t"
"v_mfma_f32_16x16x32_bf16 v[80:83], v[124:127], v[180:183], v[80:83]\n\t"
"v_fma_f32 v70, v220, v251, v70\n\t"
"v_fma_f32 v78, v224, v251, v78\n\t"
"v_fma_f32 v71, v221, v251, v71\n\t"
"v_fma_f32 v79, v225, v251, v79\n\t"
"v_cvt_pk_bf16_f32 v68, v68, v69\n\t"
"v_cvt_pk_bf16_f32 v69, v70, v71\n\t"
"v_mfma_f32_16x16x32_bf16 v[84:87], v[124:127], v[184:187], v[84:87]\n\t"
"ds_read_b128 v[124:127], v45 offset:4096\n\t"
"v_mfma_f32_16x16x32_bf16 v[88:91], v[128:131], v[180:183], v[88:91]\n\t"
"v_cvt_pk_bf16_f32 v70, v76, v77\n\t"
"v_cvt_pk_bf16_f32 v71, v78, v79\n\t"
"v_pk_max_i16 v68, v68, 0\n\t"
"v_pk_max_i16 v69, v69, 0\n\t"
"v_pk_max_i16 v70, v70, 0\n\t"
"v_pk_max_i16 v71, v71, 0\n\t"
"v_mfma_f32_16x16x32_bf16 v[92:95], v[128:131], v[184:187], v[92:95]\n\t"
"ds_read_b128 v[128:131], v45 offset:10240\n\t"
"s_waitcnt lgkmcnt(3)\n\t"
"v_mfma_f32_16x16x32_bf16 v[96:99], v[132:135], v[156:159], v[96:99]\n\t"
"buffer_store_dwordx4 v[68:71], v34, %[rs], s42 offen offset:256 sc1\n\t"
"v_mfma_f32_16x16x32_bf16 v[100:103], v[132:135], v[160:163], v[100:103]\n\t"
"ds_read_b128 v[132:135], v45 offset:11264\n\t"
"v_mfma_f32_16x16x32_bf16 v[104:107], v[136:139], v[156:159], v[104:107]\n\t"
"v_mfma_f32_16x16x32_bf16 v[108:111], v[136:139], v[160:163], v[108:111]\n\t"
"ds_read_b128 v[136:139], v45 offset:17408\n\t"
"s_waitcnt lgkmcnt(3)\n\t"
"v_mfma_f32_16x16x32_bf16 v[96:99], v[120:123], v[164:167], v[96:99]\n\t"
"v_fma_f32 v80, v226, v250, v80\n\t"
"v_fma_f32 v88, v230, v250, v88\n\t"
"v_fma_f32 v81, v227, v250, v81\n\t"
"v_fma_f32 v89, v231, v250, v89\n\t"
"v_fma_f32 v82, v228, v250, v82\n\t"
"v_fma_f32 v90, v232, v250, v90\n\t"
"v_mfma_f32_16x16x32_bf16 v[100:103], v[120:123], v[168:171], v[100:103]\n\t"
"ds_read_b128 v[120:123], v45 offset:18432\n\t"
"v_mfma_f32_16x16x32_bf16 v[104:107], v[124:127], v[164:167], v[104:107]\n\t"
"v_fma_f32 v83, v229, v250, v83\n\t"
"v_fma_f32 v91, v233, v250, v91\n\t"
"v_cvt_pk_bf16_f32 v80, v80, v81\n\t"
"v_cvt_pk_bf16_f32 v81, v82, v83\n\t"
"v_cvt_pk_bf16_f32 v82, v88, v89\n\t"
"v_cvt_pk_bf16_f32 v83, v90, v91\n\t"
"v_mfma_f32_16x16x32_bf16 v[108:111], v[124:127], v[168:171], v[108:111]\n\t"
"ds_read_b128 v[124:127], v44 offset:59392\n\t"
"s_waitcnt lgkmcnt(3)\n\t"
"v_mfma_f32_16x16x32_bf16 v[96:99], v[128:131], v[172:175], v[96:99]\n\t"
"v_pk_max_i16 v80, v80, 0\n\t"
"v_pk_max_i16 v81, v81, 0\n\t"
"v_pk_max_i16 v82, v82, 0\n\t"
"v_pk_max_i16 v83, v83, 0\n\t"
"buffer_store_dwordx4 v[80:83], v34, %[rs], s42 offen offset:2048 sc1\n\t"
"v_mfma_f32_16x16x32_bf16 v[100:103], v[128:131], v[176:179], v[100:103]\n\t"
"ds_read_b128 v[128:131], v45 offset:5120\n\t"
"v_mfma_f32_16x16x32_bf16 v[104:107], v[132:135], v[172:175], v[104:107]\n\t"
"v_fma_f32 v84, v226, v251, v84\n\t"
"v_fma_f32 v92, v230, v251, v92\n\t"
"v_fma_f32 v85, v227, v251, v85\n\t"
"v_fma_f32 v93, v231, v251, v93\n\t"
"v_fma_f32 v86, v228, v251, v86\n\t"
"v_fma_f32 v94, v232, v251, v94\n\t"
"v_mfma_f32_16x16x32_bf16 v[108:111], v[132:135], v[176:179], v[108:111]\n\t"
"ds_read_b128 v[132:135], v45 offset:12288\n\t"
"s_waitcnt lgkmcnt(3)\n\t"
"v_mfma_f32_16x16x32_bf16 v[96:99], v[136:139], v[180:183], v[96:99]\n\t"
"v_fma_f32 v87, v229, v251, v87\n\t"
"v_fma_f32 v95, v233, v251, v95\n\t"
"v_cvt_pk_bf16_f32 v84, v84, v85\n\t"
"v_cvt_pk_bf16_f32 v85, v86, v87\n\t"
"v_cvt_pk_bf16_f32 v86, v92, v93\n\t"
"v_cvt_pk_bf16_f32 v87, v94, v95\n\t"
"v_mfma_f32_16x16x32_bf16 v[100:103], v[136:139], v[184:187], v[100:103]\n\t"
"ds_read_b128 v[136:139], v45 offset:19456\n\t"
"v_mfma_f32_16x16x32_bf16 v[104:107], v[120:123], v[180:183], v[104:107]\n\t"
"v_pk_max_i16 v84, v84, 0\n\t"
"v_pk_max_i16 v85, v85, 0\n\t"
"v_pk_max_i16 v86, v86, 0\n\t"
"v_pk_max_i16 v87, v87, 0\n\t"
"buffer_store_dwordx4 v[84:87], v34, %[rs], s42 offen offset:2304 sc1\n\t"
"v_mfma_f32_16x16x32_bf16 v[108:111], v[120:123], v[184:187], v[108:111]\n\t"
"s_waitcnt lgkmcnt(2)\n\t"
"v_mfma_f32_16x16x32_bf16 v[112:115], v[124:127], v[156:159], v[112:115]\n\t"
"v_mfma_f32_16x16x32_bf16 v[116:119], v[124:127], v[160:163], v[116:119]\n\t"
"v_mfma_f32_16x16x32_bf16 v[112:115], v[128:131], v[164:167], v[112:115]\n\t"
"v_mfma_f32_16x16x32_bf16 v[116:119], v[128:131], v[168:171], v[116:119]\n\t"
"s_waitcnt lgkmcnt(0)\n\t"
"v_mfma_f32_16x16x32_bf16 v[112:115], v[132:135], v[172:175], v[112:115]\n\t"
"s_add_u32 s42, s42, 4096\n\t"
"v_fma_f32 v96, v234, v250, v96\n\t"
"v_fma_f32 v104, v238, v250, v104\n\t"
"v_fma_f32 v97, v235, v250, v97\n\t"
"v_fma_f32 v105, v239, v250, v105\n\t"
"v_mfma_f32_16x16x32_bf16 v[116:119], v[132:135], v[176:179], v[116:119]\n\t"
"v_mfma_f32_16x16x32_bf16 v[112:115], v[136:139], v[180:183], v[112:115]\n\t"
"v_fma_f32 v98, v236, v250, v98\n\t"
"v_fma_f32 v106, v240, v250, v106\n\t"
"v_fma_f32 v99, v237, v250, v99\n\t"
"v_fma_f32 v107, v241, v250, v107\n\t"
"v_cvt_pk_bf16_f32 v96, v96, v97\n\t"
"v_cvt_pk_bf16_f32 v97, v98, v99\n\t"
"v_mfma_f32_16x16x32_bf16 v[116:119], v[136:139], v[184:187], v[116:119]\n\t"
"s_barrier\n\t"
"v_cvt_pk_bf16_f32 v98, v104, v105\n\t"
"v_cvt_pk_bf16_f32 v99, v106, v107\n\t"
"v_pk_max_i16 v96, v96, 0\n\t"
"v_pk_max_i16 v97, v97, 0\n\t"
"v_pk_max_i16 v98, v98, 0\n\t"
"v_pk_max_i16 v99, v99, 0\n\t"
"buffer_store_dwordx4 v[96:99], v34, %[rs], s42 offen offset:0 sc1\n\t"
"v_fma_f32 v100, v234, v251, v100\n\t"
"v_fma_f32 v108, v238, v251, v108\n\t"
"v_fma_f32 v101, v235, v251, v101\n\t"
"v_fma_f32 v109, v239, v251, v109\n\t"
"v_fma_f32 v102, v236, v251, v102\n\t"
"v_fma_f32 v110, v240, v251, v110\n\t"
"v_fma_f32 v103, v237, v251, v103\n\t"
"v_fma_f32 v111, v241, v251, v111\n\t"
"v_cvt_pk_bf16_f32 v100, v100, v101\n\t"
"v_cvt_pk_bf16_f32 v101, v102, v103\n\t"
"v_cvt_pk_bf16_f32 v102, v108, v109\n\t"
"v_cvt_pk_bf16_f32 v103, v110, v111\n\t"
"v_pk_max_i16 v100, v100, 0\n\t"
"v_pk_max_i16 v101, v101, 0\n\t"
"v_pk_max_i16 v102, v102, 0\n\t"
"v_pk_max_i16 v103, v103, 0\n\t"
"buffer_store_dwordx4 v[100:103], v34, %[rs], s42 offen offset:256 sc1\n\t"
"s_nop 3\n\t"
"v_fma_f32 v112, v242, v250, v112\n\t"
"v_fma_f32 v113, v243, v250, v113\n\t"
"v_fma_f32 v114, v244, v250, v114\n\t"
"v_fma_f32 v115, v245, v250, v115\n\t"
"v_cvt_pk_bf16_f32 v112, v112, v113\n\t"
"v_cvt_pk_bf16_f32 v113, v114, v115\n\t"
"v_pk_max_i16 v112, v112, 0\n\t"
"v_pk_max_i16 v113, v113, 0\n\t"
"buffer_store_dwordx2 v[112:113], v40, %[rs], s42 offen offset:2048 sc1\n\t"
"v_fma_f32 v116, v242, v251, v116\n\t"
"v_fma_f32 v117, v243, v251, v117\n\t"
"v_fma_f32 v118, v244, v251, v118\n\t"
"v_fma_f32 v119, v245, v251, v119\n\t"
"v_cvt_pk_bf16_f32 v116, v116, v117\n\t"
"v_cvt_pk_bf16_f32 v117, v118, v119\n\t"
"v_pk_max_i16 v116, v116, 0\n\t"
"v_pk_max_i16 v117, v117, 0\n\t"
"buffer_store_dwordx2 v[116:117], v40, %[rs], s42 offen offset:2304 sc1\n\t"
"s_add_u32 s49, s40, 98304\n\t"
"s_add_u32 m0, s38, 98304\n\t"
"s_nop 0\n\t"
"buffer_load_dwordx4 v46, %[rs], s49 offen sc1 lds\n\t"
"s_add_u32 s49, s40, 102400\n\t"
"s_add_u32 m0, s38, 102400\n\t"
"s_nop 0\n\t"
"buffer_load_dwordx4 v46, %[rs], s49 offen sc1 lds\n\t"
"s_add_u32 s49, s40, 106496\n\t"
"s_add_u32 m0, s38, 106496\n\t"
"s_nop 0\n\t"
"buffer_load_dwordx4 v46, %[rs], s49 offen sc1 lds\n\t"
"s_add_u32 s49, s40, 110592\n\t"
"s_add_u32 m0, s38, 110592\n\t"
"s_nop 0\n\t"
"buffer_load_dwordx4 v46, %[rs], s49 offen sc1 lds\n\t"
"s_add_u32 s49, s40, 114688\n\t"
"s_add_u32 m0, s38, 114688\n\t"
"s_nop 0\n\t"
"buffer_load_dwordx4 v46, %[rs], s49 offen sc1 lds\n\t"
"s_add_u32 s49, s40, 118784\n\t"
"s_add_u32 m0, s38, 118784\n\t"
"s_nop 0\n\t"
"buffer_load_dwordx4 v46, %[rs], s49 offen sc1 lds\n\t"
"s_add_u32 s49, s40, 122880\n\t"
"s_add_u32 m0, s38, 122880\n\t"
"s_nop 0\n\t"
"buffer_load_dwordx4 v46, %[rs], s49 offen sc1 lds\n\t"
"s_add_u32 s49, s40, 126976\n\t"
"s_add_u32 m0, s38, 126976\n\t"
"s_nop 0\n\t"
"buffer_load_dwordx4 v46, %[rs], s49 offen sc1 lds\n\t"
"s_waitcnt vmcnt(8)\n\t"
"buffer_store_dwordx2 v[32:33], v38, %[rs], s43 offen sc1\n\t"

                   : "+{s[36:39]}"(b1_S0), "+{s[40:43]}"(b1_S1), "+{s[44:47]}"(b1_S2), "+{v[32:35]}"(b1_V0), "+{v[36:39]}"(b1_V1), "+{v[40:43]}"(b1_V2)
                   : [rs] "s"(rs)
                   : "memory", "scc", "vcc", "s48","s49","s50","v44","v45","v46","v64","v65","v66","v67","v68","v69","v70","v71","v72","v73","v74","v75","v76","v77","v78","v79","v80","v81","v82","v83","v84","v85","v86","v87","v88","v89","v90","v91","v92","v93","v94","v95","v96","v97","v98","v99","v100","v101","v102","v103","v104","v105","v106","v107","v108","v109","v110","v111","v112","v113","v114","v115","v116","v117","v118","v119","v120","v121","v122","v123","v124","v125","v126","v127","v128","v129","v130","v131","v132","v133","v134","v135","v136","v137","v138","v139","v140","v141","v142","v143","v144","v145","v146","v147","v148","v149","v150","v151","v152","v153","v154","v155","v156","v157","v158","v159","v160","v161","v162","v163","v164","v165","v166","v167","v168","v169","v170","v171","v172","v173","v174","v175","v176","v177","v178","v179","v180","v181","v182","v183","v184","v185","v186","v187","v188","v189","v190","v191","v192","v193","v194","v195","v196","v197","v198","v199","v200","v201","v202","v203","v204","v205","v206","v207","v208","v209","v210","v211","v212","v213","v214","v215","v216","v217","v218","v219","v220","v221","v222","v223","v224","v225","v226","v227","v228","v229","v230","v231","v232","v233","v234","v235","v236","v237","v238","v239","v240","v241","v242","v243","v244","v245","v246","v247","v248","v249","v250","v251","v252","v253", ACLOB);
    }
    {
      PHASE_VARS();
      const v2u ev = {ep0, ep1};
      const int db = 4 * half + (c16 & 3), th = w & 1, kh = w >> 1;
      const int pt = 4 * rb + 2 * ((c16 >> 2) & 1) + th;
      const unsigned w2src = OFF_W2T + db * W2T_SL, flh = fline(ly, FL_H, (pt * 2 + kh) * 2 + half);
      lin_pieces(4 * 32 + w, 38 - 32, 149, lane16, rs, w2src);
      const unsigned b2_sp = OFF_H + ly * H_B + (unsigned)(pt * 216 + kh * 108) * 512u, b2_sw = w2src;
      const unsigned b2_sm = (w == 0 ? 0 : w == 1 ? 8 : w == 2 ? 15 : 22) * 1024u;
      const unsigned b2_vp0 = q * 512 + r * 16 + kh * 256, b2_vp1 = b2_vp0 ^ 256, b2_l0 = kh * 5120 + lane16, b2_l1 = b2_l0 + 61440, b2_l2 = b2_l0 + 122880, b2_vd = b2_sm + lane16;
      const unsigned b2_wq = w * 512, b2_ss = OFF_ST1 + ly * ST1_B + (unsigned)(pt * 32 + 16 * kh) * 8u, b2_vs = q * 81920 + r * 8;
      const unsigned xo = (unsigned)(pt * 80 + 10 * db) * 512u;
      const unsigned vpair = q * 512 + (16 * kh + r) * 16, vsing = 4096 + (q >> 1) * 512 + (16 * kh + r) * 16 + (q & 1) * 8;
      v4u ra0, ra1; v2u ra2; v8u s8; v2u s2;
      asm volatile("buffer_load_dwordx4 %0, %5, %8, %9 offen sc1\n\tbuffer_load_dwordx4 %1, %5, %8, %9 offen offset:2048 sc1\n\tbuffer_load_dwordx2 %2, %6, %8, %9 offen sc1\n\t"
                   "buffer_load_dwordx2 v[48:49], %7, %8, %10 offen sc1\n\tbuffer_load_dwordx2 v[50:51], %7, %8, %11 offen sc1\n\tbuffer_load_dwordx2 v[52:53], %7, %8, %12 offen sc1\n\t"
                   "buffer_load_dwordx2 v[54:55], %7, %8, %13 offen sc1\n\tbuffer_load_dwordx2 v[56:57], %7, %8, %14 offen sc1"
                   : "=&{v[248:251]}"(ra0), "=&{v[188:191]}"(ra1), "=&{v[252:253]}"(ra2), "=&{v[48:55]}"(s8), "=&{v[56:57]}"(s2)
                   : "v"(vpair), "v"(vsing), "v"(b2_vs), "s"(rs), "s"(OFF_X1 + ly * XU_B + xo), "s"(b2_ss), "s"(b2_ss + 16384), "s"(b2_ss + 32768), "s"(b2_ss + 49152), "s"(b2_ss + 65536)
                   : "memory");
      f32x16 g0, g1, g2;
      asm volatile("buffer_load_dwordx4 v[200:203], %[vd], %[rs], %[s0] offen offset:0 sc1\n\t"
"buffer_load_dwordx4 v[204:207], %[vd], %[rs], %[s0] offen offset:1024 sc1\n\t"
"buffer_load_dwordx4 v[208:211], %[vd], %[rs], %[s0] offen offset:2048 sc1\n\t"
"buffer_load_dwordx4 v[212:215], %[vd], %[rs], %[s0] offen offset:3072 sc1\n\t"
"buffer_load_dwordx4 v[216:219], %[vd], %[rs], %[s1] offen offset:0 sc1\n\t"
"buffer_load_dwordx4 v[220:223], %[vd], %[rs], %[s1] offen offset:1024 sc1\n\t"
"buffer_load_dwordx4 v[224:227], %[vd], %[rs], %[s1] offen offset:2048 sc1\n\t"
"buffer_load_dwordx4 v[228:231], %[vd], %[rs], %[s1] offen offset:3072 sc1\n\t"
"buffer_load_dwordx4 v[232:235], %[vd], %[rs], %[s2] offen offset:0 sc1\n\t"
"buffer_load_dwordx4 v[236:239], %[vd], %[rs], %[s2] offen offset:1024 sc1\n\t"
"buffer_load_dwordx4 v[240:243], %[vd], %[rs], %[s2] offen offset:2048 sc1\n\t"
"buffer_load_dwordx4 v[244:247], %[vd], %[rs], %[s2] offen offset:3072 sc1\n\t"

                   : "=&{v[200:215]}"(g0), "=&{v[216:231]}"(g1), "=&{v[232:247]}"(g2)
                   : [vd] "v"(b2_vd), [rs] "s"(rs), [s0] "s"(w2src + 5 * 30720), [s1] "s"(w2src + 5 * 30720 + 4096), [s2] "s"(w2src + 6 * 30720) : "memory");
      const rsrc_t rO = __builtin_amdgcn_make_buffer_rsrc(ly ? (void*)p.out : (void*)p.ws, 0, ly ? 2048 * 640 * 2 : WS_USED, 0x00020000);
      const unsigned orow = (unsigned)(32 * pt + sigma(16 * kh + r)) * 1280u + db * 160;
      const unsigned b2_rw = 122880 + w * 5120 + lane16, b2_rr = 122880 + (w ^ 2) * 5120 + lane16;
      const unsigned b2_o0 = ly ? orow + q * 16 : vpair, b2_o1 = ly ? orow + 64 + q * 16 : vpair + 2048, b2_o2 = ly ? orow + 128 + q * 8 : vsing;
      const unsigned b2_so = ly ? 0 : OFF_XA + XU_B + xo, b2_ly = ly, b2_to = OFF_STA + STA_B + (unsigned)(db * 2048 + pt * 32 + 16 * kh) * 8u;
      const unsigned b2_fx = fline(1, FL_X, pt * 2) + (db * 2 + kh) * 8, b2_vt = lane < 16 ? r * 8u : OOB, b2_v2 = lane < 2 ? lane * 128u : OOB;
      v4u b2_S0 = {(unsigned)(b2_sp), (unsigned)(b2_sw), (unsigned)(b2_sm), (unsigned)(b2_wq)}; v4u b2_S1 = {(unsigned)(b2_so), (unsigned)(b2_ly), (unsigned)(b2_to), (unsigned)(b2_fx)}; v4u b2_V0 = {ep0, ep1, (unsigned)(b2_vp0), (unsigned)(b2_vp1)}; v4u b2_V1 = {(unsigned)(b2_l0), (unsigned)(b2_l1), (unsigned)(b2_l2), (unsigned)(b2_vd)}; v4u b2_V2 = {(unsigned)(b2_rw), (unsigned)(b2_rr), (unsigned)(b2_o0), (unsigned)(b2_o1)}; v4u b2_V3 = {(unsigned)(b2_o2), (unsigned)(b2_vt), (unsigned)(b2_v2), (unsigned)(b2_v2)};
      asm volatile("" : "+{s[36:39]}"(b2_S0), "+{s[40:43]}"(b2_S1));
      asm volatile("" : "+{v[32:35]}"(b2_V0), "+{v[36:39]}"(b2_V1), "+{v[40:43]}"(b2_V2), "+{v[44:47]}"(b2_V3));
      poll_spin((lane & 7) * 8, rs, flh, ev);
      asm volatile("s_add_u32 s44, s36, 0\n\t"
"buffer_load_dwordx4 v[124:127], v34, %[rs], s44 offen sc1\n\t"
"buffer_load_dwordx4 v[128:131], v35, %[rs], s44 offen sc1\n\t"
"s_add_u32 s44, s36, 2048\n\t"
"buffer_load_dwordx4 v[132:135], v34, %[rs], s44 offen sc1\n\t"
"buffer_load_dwordx4 v[136:139], v35, %[rs], s44 offen sc1\n\t"
"s_add_u32 s44, s36, 4096\n\t"
"buffer_load_dwordx4 v[140:143], v34, %[rs], s44 offen sc1\n\t"
"buffer_load_dwordx4 v[144:147], v35, %[rs], s44 offen sc1\n\t"
"s_add_u32 s44, s36, 6144\n\t"
"buffer_load_dwordx4 v[148:151], v34, %[rs], s44 offen sc1\n\t"
"buffer_load_dwordx4 v[152:155], v35, %[rs], s44 offen sc1\n\t"
"s_add_u32 s44, s36, 8192\n\t"
"buffer_load_dwordx4 v[156:159], v34, %[rs], s44 offen sc1\n\t"
"buffer_load_dwordx4 v[160:163], v35, %[rs], s44 offen sc1\n\t"
"s_add_u32 s44, s36, 10240\n\t"
"buffer_load_dwordx4 v[164:167], v34, %[rs], s44 offen sc1\n\t"
"buffer_load_dwordx4 v[168:171], v35, %[rs], s44 offen sc1\n\t"
"s_waitcnt vmcnt(12)\n\t"
"s_barrier\n\t"
"ds_read_b128 v[104:107], v36 offset:0\n\t"
"ds_read_b128 v[108:111], v36 offset:1024\n\t"
"ds_read_b128 v[112:115], v36 offset:2048\n\t"
"ds_read_b128 v[116:119], v36 offset:3072\n\t"
"ds_read_b128 v[120:123], v36 offset:4096\n\t"
"v_mov_b32 v64, 0\n\t"
"v_mov_b32 v65, 0\n\t"
"v_mov_b32 v66, 0\n\t"
"v_mov_b32 v67, 0\n\t"
"v_mov_b32 v68, 0\n\t"
"v_mov_b32 v69, 0\n\t"
"v_mov_b32 v70, 0\n\t"
"v_mov_b32 v71, 0\n\t"
"v_mov_b32 v72, 0\n\t"
"v_mov_b32 v73, 0\n\t"
"v_mov_b32 v74, 0\n\t"
"v_mov_b32 v75, 0\n\t"
"v_mov_b32 v76, 0\n\t"
"v_mov_b32 v77, 0\n\t"
"v_mov_b32 v78, 0\n\t"
"v_mov_b32 v79, 0\n\t"
"v_mov_b32 v80, 0\n\t"
"v_mov_b32 v81, 0\n\t"
"v_mov_b32 v82, 0\n\t"
"v_mov_b32 v83, 0\n\t"
"v_mov_b32 v84, 0\n\t"
"v_mov_b32 v85, 0\n\t"
"v_mov_b32 v86, 0\n\t"
"v_mov_b32 v87, 0\n\t"
"v_mov_b32 v88, 0\n\t"
"v_mov_b32 v89, 0\n\t"
"v_mov_b32 v90, 0\n\t"
"v_mov_b32 v91, 0\n\t"
"v_mov_b32 v92, 0\n\t"
"v_mov_b32 v93, 0\n\t"
"v_mov_b32 v94, 0\n\t"
"v_mov_b32 v95, 0\n\t"
"v_mov_b32 v96, 0\n\t"
"v_mov_b32 v97, 0\n\t"
"v_mov_b32 v98, 0\n\t"
"v_mov_b32 v99, 0\n\t"
"v_mov_b32 v100, 0\n\t"
"v_mov_b32 v101, 0\n\t"
"v_mov_b32 v102, 0\n\t"
"v_mov_b32 v103, 0\n\t"
"s_waitcnt vmcnt(10) lgkmcnt(3)\n\t"
"v_mfma_f32_16x16x32_bf16 v[64:67], v[104:107], v[124:127], v[64:67]\n\t"
"s_add_u32 s44, s36, 12288\n\t"
"buffer_load_dwordx4 v[172:175], v34, %[rs], s44 offen sc1\n\t"
"v_mfma_f32_16x16x32_bf16 v[68:71], v[104:107], v[128:131], v[68:71]\n\t"
"ds_read_b128 v[104:107], v36 offset:10240\n\t"
"v_mfma_f32_16x16x32_bf16 v[72:75], v[108:111], v[124:127], v[72:75]\n\t"
"buffer_load_dwordx4 v[176:179], v35, %[rs], s44 offen sc1\n\t"
"v_mfma_f32_16x16x32_bf16 v[76:79], v[108:111], v[128:131], v[76:79]\n\t"
"ds_read_b128 v[108:111], v36 offset:11264\n\t"
"s_waitcnt lgkmcnt(3)\n\t"
"v_mfma_f32_16x16x32_bf16 v[80:83], v[112:115], v[124:127], v[80:83]\n\t"
"s_add_u32 s44, s36, 14336\n\t"
"buffer_load_dwordx4 v[180:183], v34, %[rs], s44 offen sc1\n\t"
"v_mfma_f32_16x16x32_bf16 v[84:87], v[112:115], v[128:131], v[84:87]\n\t"
"ds_read_b128 v[112:115], v36 offset:12288\n\t"
"v_mfma_f32_16x16x32_bf16 v[88:91], v[116:119], v[124:127], v[88:91]\n\t"
"buffer_load_dwordx4 v[184:187], v35, %[rs], s44 offen sc1\n\t"
"v_mfma_f32_16x16x32_bf16 v[92:95], v[116:119], v[128:131], v[92:95]\n\t"
"ds_read_b128 v[116:119], v36 offset:13312\n\t"
"s_waitcnt lgkmcnt(3)\n\t"
"v_mfma_f32_16x16x32_bf16 v[96:99], v[120:123], v[124:127], v[96:99]\n\t"
"v_mfma_f32_16x16x32_bf16 v[100:103], v[120:123], v[128:131], v[100:103]\n\t"
"ds_read_b128 v[120:123], v36 offset:14336\n\t"
"s_waitcnt vmcnt(12)\n\t"
"v_mfma_f32_16x16x32_bf16 v[64:67], v[104:107], v[132:135], v[64:67]\n\t"
"v_mfma_f32_16x16x32_bf16 v[68:71], v[104:107], v[136:139], v[68:71]\n\t"
"ds_read_b128 v[104:107], v36 offset:20480\n\t"
"s_waitcnt lgkmcnt(3)\n\t"
"v_mfma_f32_16x16x32_bf16 v[72:75], v[108:111], v[132:135], v[72:75]\n\t"
"s_add_u32 s44, s36, 16384\n\t"
"buffer_load_dwordx4 v[124:127], v34, %[rs], s44 offen sc1\n\t"
"v_mfma_f32_16x16x32_bf16 v[76:79], v[108:111], v[136:139], v[76:79]\n\t"
"ds_read_b128 v[108:111], v36 offset:21504\n\t"
"v_mfma_f32_16x16x32_bf16 v[80:83], v[112:115], v[132:135], v[80:83]\n\t"
"v_mfma_f32_16x16x32_bf16 v[84:87], v[112:115], v[136:139], v[84:87]\n\t"
"ds_read_b128 v[112:115], v36 offset:22528\n\t"
"s_waitcnt lgkmcnt(3)\n\t"
"v_mfma_f32_16x16x32_bf16 v[88:91], v[116:119], v[132:135], v[88:91]\n\t"
"buffer_load_dwordx4 v[128:131], v35, %[rs], s44 offen sc1\n\t"
"v_mfma_f32_16x16x32_bf16 v[92:95], v[116:119], v[136:139], v[92:95]\n\t"
"ds_read_b128 v[116:119], v36 offset:23552\n\t"
"v_mfma_f32_16x16x32_bf16 v[96:99], v[120:123], v[132:135], v[96:99]\n\t"
"v_mfma_f32_16x16x32_bf16 v[100:103], v[120:123], v[136:139], v[100:103]\n\t"
"ds_read_b128 v[120:123], v36 offset:24576\n\t"
"s_waitcnt vmcnt(12) lgkmcnt(3)\n\t"
"v_mfma_f32_16x16x32_bf16 v[64:67], v[104:107], v[140:143], v[64:67]\n\t"
"v_mfma_f32_16x16x32_bf16 v[68:71], v[104:107], v[144:147], v[68:71]\n\t"
"ds_read_b128 v[104:107], v36 offset:30720\n\t"
"v_mfma_f32_16x16x32_bf16 v[72:75], v[108:111], v[140:143], v[72:75]\n\t"
"s_add_u32 s44, s36, 18432\n\t"
"buffer_load_dwordx4 v[132:135], v34, %[rs], s44 offen sc1\n\t"
"v_mfma_f32_16x16x32_bf16 v[76:79], v[108:111], v[144:147], v[76:79]\n\t"
"ds_read_b128 v[108:111], v36 offset:31744\n\t"
"s_waitcnt lgkmcnt(3)\n\t"
"v_mfma_f32_16x16x32_bf16 v[80:83], v[112:115], v[140:143], v[80:83]\n\t"
"v_mfma_f32_16x16x32_bf16 v[84:87], v[112:115], v[144:147], v[84:87]\n\t"
"ds_read_b128 v[112:115], v36 offset:32768\n\t"
"v_mfma_f32_16x16x32_bf16 v[88:91], v[116:119], v[140:143], v[88:91]\n\t"
"buffer_load_dwordx4 v[136:139], v35, %[rs], s44 offen sc1\n\t"
"v_mfma_f32_16x16x32_bf16 v[92:95], v[116:119], v[144:147], v[92:95]\n\t"
"ds_read_b128 v[116:119], v36 offset:33792\n\t"
"s_waitcnt lgkmcnt(3)\n\t"
"v_mfma_f32_16x16x32_bf16 v[96:99], v[120:123], v[140:143], v[96:99]\n\t"
"v_mfma_f32_16x16x32_bf16 v[100:103], v[120:123], v[144:147], v[100:103]\n\t"
"ds_read_b128 v[120:123], v36 offset:34816\n\t"
"s_waitcnt vmcnt(12)\n\t"
"v_mfma_f32_16x16x32_bf16 v[64:67], v[104:107], v[148:151], v[64:67]\n\t"
"v_mfma_f32_16x16x32_bf16 v[68:71], v[104:107], v[152:155], v[68:71]\n\t"
"ds_read_b128 v[104:107], v36 offset:40960\n\t"
"s_waitcnt lgkmcnt(3)\n\t"
"v_mfma_f32_16x16x32_bf16 v[72:75], v[108:111], v[148:151], v[72:75]\n\t"
"s_add_u32 s44, s36, 20480\n\t"
"buffer_load_dwordx4 v[140:143], v34, %[rs], s44 offen sc1\n\t"
"v_mfma_f32_16x16x32_bf16 v[76:79], v[108:111], v[152:155], v[76:79]\n\t"
"ds_read_b128 v[108:111], v36 offset:41984\n\t"
"v_mfma_f32_16x16x32_bf16 v[80:83], v[112:115], v[148:151], v[80:83]\n\t"
"v_mfma_f32_16x16x32_bf16 v[84:87], v[112:115], v[152:155], v[84:87]\n\t"
"ds_read_b128 v[112:115], v36 offset:43008\n\t"
"s_waitcnt lgkmcnt(3)\n\t"
"v_mfma_f32_16x16x32_bf16 v[88:91], v[116:119], v[148:151], v[88:91]\n\t"
"buffer_load_dwordx4 v[144:147], v35, %[rs], s44 offen sc1\n\t"
"v_mfma_f32_16x16x32_bf16 v[92:95], v[116:119], v[152:155], v[92:95]\n\t"
"ds_read_b128 v[116:119], v36 offset:44032\n\t"
"v_mfma_f32_16x16x32_bf16 v[96:99], v[120:123], v[148:151], v[96:99]\n\t"
"v_mfma_f32_16x16x32_bf16 v[100:103], v[120:123], v[152:155], v[100:103]\n\t"
"ds_read_b128 v[120:123], v36 offset:45056\n\t"
"s_waitcnt vmcnt(12) lgkmcnt(3)\n\t"
"v_mfma_f32_16x16x32_bf16 v[64:67], v[104:107], v[156:159], v[64:67]\n\t"
"v_mfma_f32_16x16x32_bf16 v[68:71], v[104:107], v[160:163], v[68:71]\n\t"
"ds_read_b128 v[104:107], v36 offset:51200\n\t"
"v_mfma_f32_16x16x32_bf16 v[72:75], v[108:111], v[156:159], v[72:75]\n\t"
"s_add_u32 s44, s36, 22528\n\t"
"buffer_load_dwordx4 v[148:151], v34, %[rs], s44 offen sc1\n\t"
"v_mfma_f32_16x16x32_bf16 v[76:79], v[108:111], v[160:163], v[76:79]\n\t"
"ds_read_b128 v[108:111], v36 offset:52224\n\t"
"s_waitcnt lgkmcnt(3)\n\t"
"v_mfma_f32_16x16x32_bf16 v[80:83], v[112:115], v[156:159], v[80:83]\n\t"
"v_mfma_f32_16x16x32_bf16 v[84:87], v[112:115], v[160:163], v[84:87]\n\t"
"ds_read_b128 v[112:115], v36 offset:53248\n\t"
"v_mfma_f32_16x16x32_bf16 v[88:91], v[116:119], v[156:159], v[88:91]\n\t"
"buffer_load_dwordx4 v[152:155], v35, %[rs], s44 offen sc1\n\t"
"v_mfma_f32_16x16x32_bf16 v[92:95], v[116:119], v[160:163], v[92:95]\n\t"
"ds_read_b128 v[116:119], v36 offset:54272\n\t"
"s_waitcnt lgkmcnt(3)\n\t"
"v_mfma_f32_16x16x32_bf16 v[96:99], v[120:123], v[156:159], v[96:99]\n\t"
"v_mfma_f32_16x16x32_bf16 v[100:103], v[120:123], v[160:163], v[100:103]\n\t"
"ds_read_b128 v[120:123], v36 offset:55296\n\t"
"s_waitcnt vmcnt(12)\n\t"
"v_mfma_f32_16x16x32_bf16 v[64:67], v[104:107], v[164:167], v[64:67]\n\t"
"s_barrier\n\t"
"v_mfma_f32_16x16x32_bf16 v[68:71], v[104:107], v[168:171], v[68:71]\n\t"
"ds_read_b128 v[104:107], v37 offset:0\n\t"
"s_waitcnt lgkmcnt(3)\n\t"
"v_mfma_f32_16x16x32_bf16 v[72:75], v[108:111], v[164:167], v[72:75]\n\t"
"s_add_u32 s44, s36, 24576\n\t"
"buffer_load_dwordx4 v[156:159], v34, %[rs], s44 offen sc1\n\t"
"v_mfma_f32_16x16x32_bf16 v[76:79], v[108:111], v[168:171], v[76:79]\n\t"
"ds_read_b128 v[108:111], v37 offset:1024\n\t"
"v_mfma_f32_16x16x32_bf16 v[80:83], v[112:115], v[164:167], v[80:83]\n\t"
"ds_write_b128 v39, v[200:203] offset:0\n\t"
"ds_write_b128 v39, v[204:207] offset:1024\n\t"
"ds_write_b128 v39, v[208:211] offset:2048\n\t"
"v_mfma_f32_16x16x32_bf16 v[84:87], v[112:115], v[168:171], v[84:87]\n\t"
"ds_read_b128 v[112:115], v37 offset:2048\n\t"
"s_waitcnt lgkmcnt(6)\n\t"
"v_mfma_f32_16x16x32_bf16 v[88:91], v[116:119], v[164:167], v[88:91]\n\t"
"buffer_load_dwordx4 v[160:163], v35, %[rs], s44 offen sc1\n\t"
"v_mfma_f32_16x16x32_bf16 v[92:95], v[116:119], v[168:171], v[92:95]\n\t"
"ds_read_b128 v[116:119], v37 offset:3072\n\t"
"v_mfma_f32_16x16x32_bf16 v[96:99], v[120:123], v[164:167], v[96:99]\n\t"
"ds_write_b128 v39, v[212:215] offset:3072\n\t"
"ds_write_b128 v39, v[216:219] offset:4096\n\t"
"ds_write_b128 v39, v[220:223] offset:5120\n\t"
"v_mfma_f32_16x16x32_bf16 v[100:103], v[120:123], v[168:171], v[100:103]\n\t"
"ds_read_b128 v[120:123], v37 offset:4096\n\t"
"s_waitcnt vmcnt(12) lgkmcnt(9)\n\t"
"v_mfma_f32_16x16x32_bf16 v[64:67], v[104:107], v[172:175], v[64:67]\n\t"
"ds_write_b128 v39, v[224:227] offset:6144\n\t"
"ds_write_b128 v39, v[228:231] offset:7168\n\t"
"v_mfma_f32_16x16x32_bf16 v[68:71], v[104:107], v[176:179], v[68:71]\n\t"
"ds_read_b128 v[104:107], v37 offset:10240\n\t"
"v_mfma_f32_16x16x32_bf16 v[72:75], v[108:111], v[172:175], v[72:75]\n\t"
"s_add_u32 s44, s36, 26624\n\t"
"buffer_load_dwordx4 v[164:167], v34, %[rs], s44 offen sc1\n\t"
"v_mfma_f32_16x16x32_bf16 v[76:79], v[108:111], v[176:179], v[76:79]\n\t"
"ds_read_b128 v[108:111], v37 offset:11264\n\t"
"s_waitcnt lgkmcnt(8)\n\t"
"v_mfma_f32_16x16x32_bf16 v[80:83], v[112:115], v[172:175], v[80:83]\n\t"
"v_mfma_f32_16x16x32_bf16 v[84:87], v[112:115], v[176:179], v[84:87]\n\t"
"ds_read_b128 v[112:115], v37 offset:12288\n\t"
"v_mfma_f32_16x16x32_bf16 v[88:91], v[116:119], v[172:175], v[88:91]\n\t"
"buffer_load_dwordx4 v[168:171], v35, %[rs], s44 offen sc1\n\t"
"v_mfma_f32_16x16x32_bf16 v[92:95], v[116:119], v[176:179], v[92:95]\n\t"
"ds_read_b128 v[116:119], v37 offset:13312\n\t"
"s_waitcnt lgkmcnt(3)\n\t"
"v_mfma_f32_16x16x32_bf16 v[96:99], v[120:123], v[172:175], v[96:99]\n\t"
"v_mfma_f32_16x16x32_bf16 v[100:103], v[120:123], v[176:179], v[100:103]\n\t"
"ds_read_b128 v[120:123], v37 offset:14336\n\t"
"s_waitcnt vmcnt(12)\n\t"
"v_mfma_f32_16x16x32_bf16 v[64:67], v[104:107], v[180:183], v[64:67]\n\t"
"v_mfma_f32_16x16x32_bf16 v[68:71], v[104:107], v[184:187], v[68:71]\n\t"
"ds_read_b128 v[104:107], v37 offset:20480\n\t"
"s_waitcnt lgkmcnt(3)\n\t"
"v_mfma_f32_16x16x32_bf16 v[72:75], v[108:111], v[180:183], v[72:75]\n\t"
"s_add_u32 s44, s36, 28672\n\t"
"buffer_load_dwordx4 v[172:175], v34, %[rs], s44 offen sc1\n\t"
"v_mfma_f32_16x16x32_bf16 v[76:79], v[108:111], v[184:187], v[76:79]\n\t"
"ds_read_b128 v[108:111], v37 offset:21504\n\t"
"v_mfma_f32_16x16x32_bf16 v[80:83], v[112:115], v[180:183], v[80:83]\n\t"
"v_mfma_f32_16x16x32_bf16 v[84:87], v[112:115], v[184:187], v[84:87]\n\t"
"ds_read_b128 v[112:115], v37 offset:22528\n\t"
"s_waitcnt lgkmcnt(3)\n\t"
"v_mfma_f32_16x16x32_bf16 v[88:91], v[116:119], v[180:183], v[88:91]\n\t"
"buffer_load_dwordx4 v[176:179], v35, %[rs], s44 offen sc1\n\t"
"v_mfma_f32_16x16x32_bf16 v[92:95], v[116:119], v[184:187], v[92:95]\n\t"
"ds_read_b128 v[116:119], v37 offset:23552\n\t"
"v_mfma_f32_16x16x32_bf16 v[96:99], v[120:123], v[180:183], v[96:99]\n\t"
"v_mfma_f32_16x16x32_bf16 v[100:103], v[120:123], v[184:187], v[100:103]\n\t"
"ds_read_b128 v[120:123], v37 offset:24576\n\t"
"s_waitcnt vmcnt(12) lgkmcnt(3)\n\t"
"v_mfma_f32_16x16x32_bf16 v[64:67], v[104:107], v[124:127], v[64:67]\n\t"
"s_barrier\n\t"
"v_mfma_f32_16x16x32_bf16 v[68:71], v[104:107], v[128:131], v[68:71]\n\t"
"ds_read_b128 v[104:107], v37 offset:30720\n\t"
"v_mfma_f32_16x16x32_bf16 v[72:75], v[108:111], v[124:127], v[72:75]\n\t"
"s_add_u32 s44, s36, 30720\n\t"
"buffer_load_dwordx4 v[180:183], v34, %[rs], s44 offen sc1\n\t"
"v_mfma_f32_16x16x32_bf16 v[76:79], v[108:111], v[128:131], v[76:79]\n\t"
"ds_read_b128 v[108:111], v37 offset:31744\n\t"
"s_waitcnt lgkmcnt(3)\n\t"
"v_mfma_f32_16x16x32_bf16 v[80:83], v[112:115], v[124:127], v[80:83]\n\t"
"s_add_u32 s45, s37, 188416\n\t"
"s_add_u32 m0, s38, 34816\n\t"
"s_nop 0\n\t"
"buffer_load_dwordx4 v39, %[rs], s45 offen offset:0 sc1 lds\n\t"
"v_mfma_f32_16x16x32_bf16 v[84:87], v[112:115], v[128:131], v[84:87]\n\t"
"ds_read_b128 v[112:115], v37 offset:32768\n\t"
"v_mfma_f32_16x16x32_bf16 v[88:91], v[116:119], v[124:127], v[88:91]\n\t"
"buffer_load_dwordx4 v[184:187], v35, %[rs], s44 offen sc1\n\t"
"v_mfma_f32_16x16x32_bf16 v[92:95], v[116:119], v[128:131], v[92:95]\n\t"
"ds_read_b128 v[116:119], v37 offset:33792\n\t"
"s_waitcnt lgkmcnt(3)\n\t"
"v_mfma_f32_16x16x32_bf16 v[96:99], v[120:123], v[124:127], v[96:99]\n\t"
"buffer_load_dwordx4 v39, %[rs], s45 offen offset:1024 sc1 lds\n\t"
"v_mfma_f32_16x16x32_bf16 v[100:103], v[120:123], v[128:131], v[100:103]\n\t"
"ds_read_b128 v[120:123], v37 offset:34816\n\t"
"s_waitcnt vmcnt(14)\n\t"
"v_mfma_f32_16x16x32_bf16 v[64:67], v[104:107], v[132:135], v[64:67]\n\t"
"buffer_load_dwordx4 v39, %[rs], s45 offen offset:2048 sc1 lds\n\t"
"v_mfma_f32_16x16x32_bf16 v[68:71], v[104:107], v[136:139], v[68:71]\n\t"
"ds_read_b128 v[104:107], v37 offset:40960\n\t"
"s_waitcnt lgkmcnt(3)\n\t"
"v_mfma_f32_16x16x32_bf16 v[72:75], v[108:111], v[132:135], v[72:75]\n\t"
"s_add_u32 s44, s36, 32768\n\t"
"buffer_load_dwordx4 v[124:127], v34, %[rs], s44 offen sc1\n\t"
"v_mfma_f32_16x16x32_bf16 v[76:79], v[108:111], v[136:139], v[76:79]\n\t"
"ds_read_b128 v[108:111], v37 offset:41984\n\t"
"v_mfma_f32_16x16x32_bf16 v[80:83], v[112:115], v[132:135], v[80:83]\n\t"
"buffer_load_dwordx4 v39, %[rs], s45 offen offset:3072 sc1 lds\n\t"
"v_mfma_f32_16x16x32_bf16 v[84:87], v[112:115], v[136:139], v[84:87]\n\t"
"ds_read_b128 v[112:115], v37 offset:43008\n\t"
"s_waitcnt lgkmcnt(3)\n\t"
"v_mfma_f32_16x16x32_bf16 v[88:91], v[116:119], v[132:135], v[88:91]\n\t"
"buffer_load_dwordx4 v[128:131], v35, %[rs], s44 offen sc1\n\t"
"v_mfma_f32_16x16x32_bf16 v[92:95], v[116:119], v[136:139], v[92:95]\n\t"
"ds_read_b128 v[116:119], v37 offset:44032\n\t"
"v_mfma_f32_16x16x32_bf16 v[96:99], v[120:123], v[132:135], v[96:99]\n\t"
"ds_write_b128 v39, v[232:235] offset:30720\n\t"
"ds_write_b128 v39, v[236:239] offset:31744\n\t"
"ds_write_b128 v39, v[240:243] offset:32768\n\t"
"v_mfma_f32_16x16x32_bf16 v[100:103], v[120:123], v[136:139], v[100:103]\n\t"
"ds_read_b128 v[120:123], v37 offset:45056\n\t"
"s_waitcnt vmcnt(16) lgkmcnt(6)\n\t"
"v_mfma_f32_16x16x32_bf16 v[64:67], v[104:107], v[140:143], v[64:67]\n\t"
"ds_write_b128 v39, v[244:247] offset:33792\n\t"
"v_mfma_f32_16x16x32_bf16 v[68:71], v[104:107], v[144:147], v[68:71]\n\t"
"ds_read_b128 v[104:107], v37 offset:51200\n\t"
"v_mfma_f32_16x16x32_bf16 v[72:75], v[108:111], v[140:143], v[72:75]\n\t"
"s_add_u32 s44, s36, 34816\n\t"
"buffer_load_dwordx4 v[132:135], v34, %[rs], s44 offen sc1\n\t"
"v_mfma_f32_16x16x32_bf16 v[76:79], v[108:111], v[144:147], v[76:79]\n\t"
"ds_read_b128 v[108:111], v37 offset:52224\n\t"
"s_waitcnt lgkmcnt(7)\n\t"
"v_mfma_f32_16x16x32_bf16 v[80:83], v[112:115], v[140:143], v[80:83]\n\t"
"v_mfma_f32_16x16x32_bf16 v[84:87], v[112:115], v[144:147], v[84:87]\n\t"
"ds_read_b128 v[112:115], v37 offset:53248\n\t"
"v_mfma_f32_16x16x32_bf16 v[88:91], v[116:119], v[140:143], v[88:91]\n\t"
"buffer_load_dwordx4 v[136:139], v35, %[rs], s44 offen sc1\n\t"
"v_mfma_f32_16x16x32_bf16 v[92:95], v[116:119], v[144:147], v[92:95]\n\t"
"ds_read_b128 v[116:119], v37 offset:54272\n\t"
"s_waitcnt lgkmcnt(3)\n\t"
"v_mfma_f32_16x16x32_bf16 v[96:99], v[120:123], v[140:143], v[96:99]\n\t"
"v_mfma_f32_16x16x32_bf16 v[100:103], v[120:123], v[144:147], v[100:103]\n\t"
"ds_read_b128 v[120:123], v37 offset:55296\n\t"
"s_waitcnt vmcnt(16)\n\t"
"v_mfma_f32_16x16x32_bf16 v[64:67], v[104:107], v[148:151], v[64:67]\n\t"
"s_barrier\n\t"
"v_mfma_f32_16x16x32_bf16 v[68:71], v[104:107], v[152:155], v[68:71]\n\t"
"ds_read_b128 v[104:107], v38 offset:0\n\t"
"s_waitcnt lgkmcnt(3)\n\t"
"v_mfma_f32_16x16x32_bf16 v[72:75], v[108:111], v[148:151], v[72:75]\n\t"
"s_add_u32 s44, s36, 36864\n\t"
"buffer_load_dwordx4 v[140:143], v34, %[rs], s44 offen sc1\n\t"
"v_mfma_f32_16x16x32_bf16 v[76:79], v[108:111], v[152:155], v[76:79]\n\t"
"ds_read_b128 v[108:111], v38 offset:1024\n\t"
"v_mfma_f32_16x16x32_bf16 v[80:83], v[112:115], v[148:151], v[80:83]\n\t"
"s_add_u32 s45, s37, 215040\n\t"
"s_add_u32 m0, s38, 61440\n\t"
"s_nop 0\n\t"
"buffer_load_dwordx4 v39, %[rs], s45 offen offset:0 sc1 lds\n\t"
"v_mfma_f32_16x16x32_bf16 v[84:87], v[112:115], v[152:155], v[84:87]\n\t"
"ds_read_b128 v[112:115], v38 offset:2048\n\t"
"s_waitcnt lgkmcnt(3)\n\t"
"v_mfma_f32_16x16x32_bf16 v[88:91], v[116:119], v[148:151], v[88:91]\n\t"
"buffer_load_dwordx4 v[144:147], v35, %[rs], s44 offen sc1\n\t"
"v_mfma_f32_16x16x32_bf16 v[92:95], v[116:119], v[152:155], v[92:95]\n\t"
"ds_read_b128 v[116:119], v38 offset:3072\n\t"
"v_mfma_f32_16x16x32_bf16 v[96:99], v[120:123], v[148:151], v[96:99]\n\t"
"buffer_load_dwordx4 v39, %[rs], s45 offen offset:1024 sc1 lds\n\t"
"v_mfma_f32_16x16x32_bf16 v[100:103], v[120:123], v[152:155], v[100:103]\n\t"
"ds_read_b128 v[120:123], v38 offset:4096\n\t"
"s_waitcnt vmcnt(18) lgkmcnt(3)\n\t"
"v_mfma_f32_16x16x32_bf16 v[64:67], v[104:107], v[156:159], v[64:67]\n\t"
"buffer_load_dwordx4 v39, %[rs], s45 offen offset:2048 sc1 lds\n\t"
"v_mfma_f32_16x16x32_bf16 v[68:71], v[104:107], v[160:163], v[68:71]\n\t"
"ds_read_b128 v[104:107], v38 offset:10240\n\t"
"v_mfma_f32_16x16x32_bf16 v[72:75], v[108:111], v[156:159], v[72:75]\n\t"
"s_add_u32 s44, s36, 38912\n\t"
"buffer_load_dwordx4 v[148:151], v34, %[rs], s44 offen sc1\n\t"
"v_mfma_f32_16x16x32_bf16 v[76:79], v[108:111], v[160:163], v[76:79]\n\t"
"ds_read_b128 v[108:111], v38 offset:11264\n\t"
"s_waitcnt lgkmcnt(3)\n\t"
"v_mfma_f32_16x16x32_bf16 v[80:83], v[112:115], v[156:159], v[80:83]\n\t"
"buffer_load_dwordx4 v39, %[rs], s45 offen offset:3072 sc1 lds\n\t"
"v_mfma_f32_16x16x32_bf16 v[84:87], v[112:115], v[160:163], v[84:87]\n\t"
"ds_read_b128 v[112:115], v38 offset:12288\n\t"
"v_mfma_f32_16x16x32_bf16 v[88:91], v[116:119], v[156:159], v[88:91]\n\t"
"buffer_load_dwordx4 v[152:155], v35, %[rs], s44 offen sc1\n\t"
"v_mfma_f32_16x16x32_bf16 v[92:95], v[116:119], v[160:163], v[92:95]\n\t"
"ds_read_b128 v[116:119], v38 offset:13312\n\t"
"s_waitcnt lgkmcnt(3)\n\t"
"v_mfma_f32_16x16x32_bf16 v[96:99], v[120:123], v[156:159], v[96:99]\n\t"
"s_add_u32 s45, s37, 219136\n\t"
"s_add_u32 m0, s38, 65536\n\t"
"s_nop 0\n\t"
"buffer_load_dwordx4 v39, %[rs], s45 offen offset:0 sc1 lds\n\t"
"v_mfma_f32_16x16x32_bf16 v[100:103], v[120:123], v[160:163], v[100:103]\n\t"
"ds_read_b128 v[120:123], v38 offset:14336\n\t"
"s_waitcnt vmcnt(21)\n\t"
"v_mfma_f32_16x16x32_bf16 v[64:67], v[104:107], v[164:167], v[64:67]\n\t"
"buffer_load_dwordx4 v39, %[rs], s45 offen offset:1024 sc1 lds\n\t"
"v_mfma_f32_16x16x32_bf16 v[68:71], v[104:107], v[168:171], v[68:71]\n\t"
"ds_read_b128 v[104:107], v38 offset:20480\n\t"
"s_waitcnt lgkmcnt(3)\n\t"
"v_mfma_f32_16x16x32_bf16 v[72:75], v[108:111], v[164:167], v[72:75]\n\t"
"s_add_u32 s44, s36, 40960\n\t"
"buffer_load_dwordx4 v[156:159], v34, %[rs], s44 offen sc1\n\t"
"v_mfma_f32_16x16x32_bf16 v[76:79], v[108:111], v[168:171], v[76:79]\n\t"
"ds_read_b128 v[108:111], v38 offset:21504\n\t"
"v_mfma_f32_16x16x32_bf16 v[80:83], v[112:115], v[164:167], v[80:83]\n\t"
"buffer_load_dwordx4 v39, %[rs], s45 offen offset:2048 sc1 lds\n\t"
"v_mfma_f32_16x16x32_bf16 v[84:87], v[112:115], v[168:171], v[84:87]\n\t"
"ds_read_b128 v[112:115], v38 offset:22528\n\t"
"s_waitcnt lgkmcnt(3)\n\t"
"v_mfma_f32_16x16x32_bf16 v[88:91], v[116:119], v[164:167], v[88:91]\n\t"
"buffer_load_dwordx4 v[160:163], v35, %[rs], s44 offen sc1\n\t"
"v_mfma_f32_16x16x32_bf16 v[92:95], v[116:119], v[168:171], v[92:95]\n\t"
"ds_read_b128 v[116:119], v38 offset:23552\n\t"
"v_mfma_f32_16x16x32_bf16 v[96:99], v[120:123], v[164:167], v[96:99]\n\t"
"buffer_load_dwordx4 v39, %[rs], s45 offen offset:3072 sc1 lds\n\t"
"v_mfma_f32_16x16x32_bf16 v[100:103], v[120:123], v[168:171], v[100:103]\n\t"
"ds_read_b128 v[120:123], v38 offset:24576\n\t"
"s_waitcnt vmcnt(24) lgkmcnt(3)\n\t"
"v_mfma_f32_16x16x32_bf16 v[64:67], v[104:107], v[172:175], v[64:67]\n\t"
"s_barrier\n\t"
"v_mfma_f32_16x16x32_bf16 v[68:71], v[104:107], v[176:179], v[68:71]\n\t"
"ds_read_b128 v[104:107], v36 offset:0\n\t"
"v_mfma_f32_16x16x32_bf16 v[72:75], v[108:111], v[172:175], v[72:75]\n\t"
"s_add_u32 s44, s36, 43008\n\t"
"buffer_load_dwordx4 v[164:167], v34, %[rs], s44 offen sc1\n\t"
"v_mfma_f32_16x16x32_bf16 v[76:79], v[108:111], v[176:179], v[76:79]\n\t"
"ds_read_b128 v[108:111], v36 offset:1024\n\t"
"s_waitcnt lgkmcnt(3)\n\t"
"v_mfma_f32_16x16x32_bf16 v[80:83], v[112:115], v[172:175], v[80:83]\n\t"
"s_add_u32 s45, s37, 245760\n\t"
"s_add_u32 m0, s38, 92160\n\t"
"s_nop 0\n\t"
"buffer_load_dwordx4 v39, %[rs], s45 offen offset:0 sc1 lds\n\t"
"v_mfma_f32_16x16x32_bf16 v[84:87], v[112:115], v[176:179], v[84:87]\n\t"
"ds_read_b128 v[112:115], v36 offset:2048\n\t"
"v_mfma_f32_16x16x32_bf16 v[88:91], v[116:119], v[172:175], v[88:91]\n\t"
"buffer_load_dwordx4 v[168:171], v35, %[rs], s44 offen sc1\n\t"
"v_mfma_f32_16x16x32_bf16 v[92:95], v[116:119], v[176:179], v[92:95]\n\t"
"ds_read_b128 v[116:119], v36 offset:3072\n\t"
"s_waitcnt lgkmcnt(3)\n\t"
"v_mfma_f32_16x16x32_bf16 v[96:99], v[120:123], v[172:175], v[96:99]\n\t"
"buffer_load_dwordx4 v39, %[rs], s45 offen offset:1024 sc1 lds\n\t"
"v_mfma_f32_16x16x32_bf16 v[100:103], v[120:123], v[176:179], v[100:103]\n\t"
"ds_read_b128 v[120:123], v36 offset:4096\n\t"
"s_waitcnt vmcnt(25)\n\t"
"v_mfma_f32_16x16x32_bf16 v[64:67], v[104:107], v[180:183], v[64:67]\n\t"
"buffer_load_dwordx4 v39, %[rs], s45 offen offset:2048 sc1 lds\n\t"
"v_mfma_f32_16x16x32_bf16 v[68:71], v[104:107], v[184:187], v[68:71]\n\t"
"ds_read_b128 v[104:107], v36 offset:10240\n\t"
"s_waitcnt lgkmcnt(3)\n\t"
"v_mfma_f32_16x16x32_bf16 v[72:75], v[108:111], v[180:183], v[72:75]\n\t"
"s_add_u32 s44, s36, 45056\n\t"
"buffer_load_dwordx4 v[172:175], v34, %[rs], s44 offen sc1\n\t"
"v_mfma_f32_16x16x32_bf16 v[76:79], v[108:111], v[184:187], v[76:79]\n\t"
"ds_read_b128 v[108:111], v36 offset:11264\n\t"
"v_mfma_f32_16x16x32_bf16 v[80:83], v[112:115], v[180:183], v[80:83]\n\t"
"buffer_load_dwordx4 v39, %[rs], s45 offen offset:3072 sc1 lds\n\t"
"v_mfma_f32_16x16x32_bf16 v[84:87], v[112:115], v[184:187], v[84:87]\n\t"
"ds_read_b128 v[112:115], v36 offset:12288\n\t"
"s_waitcnt lgkmcnt(3)\n\t"
"v_mfma_f32_16x16x32_bf16 v[88:91], v[116:119], v[180:183], v[88:91]\n\t"
"buffer_load_dwordx4 v[176:179], v35, %[rs], s44 offen sc1\n\t"
"v_mfma_f32_16x16x32_bf16 v[92:95], v[116:119], v[184:187], v[92:95]\n\t"
"ds_read_b128 v[116:119], v36 offset:13312\n\t"
"v_mfma_f32_16x16x32_bf16 v[96:99], v[120:123], v[180:183], v[96:99]\n\t"
"s_add_u32 s45, s37, 249856\n\t"
"s_add_u32 m0, s38, 96256\n\t"
"s_nop 0\n\t"
"buffer_load_dwordx4 v39, %[rs], s45 offen offset:0 sc1 lds\n\t"
"v_mfma_f32_16x16x32_bf16 v[100:103], v[120:123], v[184:187], v[100:103]\n\t"
"ds_read_b128 v[120:123], v36 offset:14336\n\t"
"s_waitcnt vmcnt(25) lgkmcnt(3)\n\t"
"v_mfma_f32_16x16x32_bf16 v[64:67], v[104:107], v[124:127], v[64:67]\n\t"
"buffer_load_dwordx4 v39, %[rs], s45 offen offset:1024 sc1 lds\n\t"
"v_mfma_f32_16x16x32_bf16 v[68:71], v[104:107], v[128:131], v[68:71]\n\t"
"ds_read_b128 v[104:107], v36 offset:20480\n\t"
"v_mfma_f32_16x16x32_bf16 v[72:75], v[108:111], v[124:127], v[72:75]\n\t"
"s_add_u32 s44, s36, 47104\n\t"
"buffer_load_dwordx4 v[180:183], v34, %[rs], s44 offen sc1\n\t"
"v_mfma_f32_16x16x32_bf16 v[76:79], v[108:111], v[128:131], v[76:79]\n\t"
"ds_read_b128 v[108:111], v36 offset:21504\n\t"
"s_waitcnt lgkmcnt(3)\n\t"
"v_mfma_f32_16x16x32_bf16 v[80:83], v[112:115], v[124:127], v[80:83]\n\t"
"buffer_load_dwordx4 v39, %[rs], s45 offen offset:2048 sc1 lds\n\t"
"v_mfma_f32_16x16x32_bf16 v[84:87], v[112:115], v[128:131], v[84:87]\n\t"
"ds_read_b128 v[112:115], v36 offset:22528\n\t"
"v_mfma_f32_16x16x32_bf16 v[88:91], v[116:119], v[124:127], v[88:91]\n\t"
"buffer_load_dwordx4 v[184:187], v35, %[rs], s44 offen sc1\n\t"
"v_mfma_f32_16x16x32_bf16 v[92:95], v[116:119], v[128:131], v[92:95]\n\t"
"ds_read_b128 v[116:119], v36 offset:23552\n\t"
"s_waitcnt lgkmcnt(3)\n\t"
"v_mfma_f32_16x16x32_bf16 v[96:99], v[120:123], v[124:127], v[96:99]\n\t"
"buffer_load_dwordx4 v39, %[rs], s45 offen offset:3072 sc1 lds\n\t"
"v_mfma_f32_16x16x32_bf16 v[100:103], v[120:123], v[128:131], v[100:103]\n\t"
"ds_read_b128 v[120:123], v36 offset:24576\n\t"
"s_waitcnt vmcnt(28)\n\t"
"v_mfma_f32_16x16x32_bf16 v[64:67], v[104:107], v[132:135], v[64:67]\n\t"
"s_barrier\n\t"
"v_mfma_f32_16x16x32_bf16 v[68:71], v[104:107], v[136:139], v[68:71]\n\t"
"ds_read_b128 v[104:107], v36 offset:30720\n\t"
"s_waitcnt lgkmcnt(3)\n\t"
"v_mfma_f32_16x16x32_bf16 v[72:75], v[108:111], v[132:135], v[72:75]\n\t"
"s_add_u32 s44, s36, 49152\n\t"
"buffer_load_dwordx4 v[124:127], v34, %[rs], s44 offen sc1\n\t"
"v_mfma_f32_16x16x32_bf16 v[76:79], v[108:111], v[136:139], v[76:79]\n\t"
"ds_read_b128 v[108:111], v36 offset:31744\n\t"
"v_mfma_f32_16x16x32_bf16 v[80:83], v[112:115], v[132:135], v[80:83]\n\t"
"v_add_f32 v60, v48, v50\n\t"
"v_add_f32 v59, v52, v54\n\t"
"v_mfma_f32_16x16x32_bf16 v[84:87], v[112:115], v[136:139], v[84:87]\n\t"
"ds_read_b128 v[112:115], v36 offset:32768\n\t"
"s_waitcnt lgkmcnt(3)\n\t"
"v_mfma_f32_16x16x32_bf16 v[88:91], v[116:119], v[132:135], v[88:91]\n\t"
"buffer_load_dwordx4 v[128:131], v35, %[rs], s44 offen sc1\n\t"
"v_mfma_f32_16x16x32_bf16 v[92:95], v[116:119], v[136:139], v[92:95]\n\t"
"ds_read_b128 v[116:119], v36 offset:33792\n\t"
"v_mfma_f32_16x16x32_bf16 v[96:99], v[120:123], v[132:135], v[96:99]\n\t"
"v_add_f32 v60, v60, v56\n\t"
"v_add_f32 v60, v60, v59\n\t"
"v_mfma_f32_16x16x32_bf16 v[100:103], v[120:123], v[136:139], v[100:103]\n\t"
"ds_read_b128 v[120:123], v36 offset:34816\n\t"
"s_waitcnt vmcnt(27) lgkmcnt(3)\n\t"
"v_mfma_f32_16x16x32_bf16 v[64:67], v[104:107], v[140:143], v[64:67]\n\t"
"v_mov_b32 v59, v60\n\t"
"s_nop 1\n\t"
"v_permlane16_swap_b32 v60, v59\n\t"
"v_add_f32 v60, v60, v59\n\t"
"v_mfma_f32_16x16x32_bf16 v[68:71], v[104:107], v[144:147], v[68:71]\n\t"
"ds_read_b128 v[104:107], v36 offset:40960\n\t"
"v_mfma_f32_16x16x32_bf16 v[72:75], v[108:111], v[140:143], v[72:75]\n\t"
"s_add_u32 s44, s36, 51200\n\t"
"buffer_load_dwordx4 v[132:135], v34, %[rs], s44 offen sc1\n\t"
"v_mfma_f32_16x16x32_bf16 v[76:79], v[108:111], v[144:147], v[76:79]\n\t"
"ds_read_b128 v[108:111], v36 offset:41984\n\t"
"s_waitcnt lgkmcnt(3)\n\t"
"v_mfma_f32_16x16x32_bf16 v[80:83], v[112:115], v[140:143], v[80:83]\n\t"
"v_mov_b32 v59, v60\n\t"
"s_nop 1\n\t"
"v_permlane32_swap_b32 v60, v59\n\t"
"v_add_f32 v60, v60, v59\n\t"
"v_mfma_f32_16x16x32_bf16 v[84:87], v[112:115], v[144:147], v[84:87]\n\t"
"ds_read_b128 v[112:115], v36 offset:43008\n\t"
"v_mfma_f32_16x16x32_bf16 v[88:91], v[116:119], v[140:143], v[88:91]\n\t"
"buffer_load_dwordx4 v[136:139], v35, %[rs], s44 offen sc1\n\t"
"v_mfma_f32_16x16x32_bf16 v[92:95], v[116:119], v[144:147], v[92:95]\n\t"
"ds_read_b128 v[116:119], v36 offset:44032\n\t"
"s_waitcnt lgkmcnt(3)\n\t"
"v_mfma_f32_16x16x32_bf16 v[96:99], v[120:123], v[140:143], v[96:99]\n\t"
"v_mul_f32 v60, 0xbd4ccccd, v60\n\t"
"v_add_f32 v48, v48, v60\n\t"
"v_add_f32 v50, v50, v60\n\t"
"v_add_f32 v52, v52, v60\n\t"
"v_mfma_f32_16x16x32_bf16 v[100:103], v[120:123], v[144:147], v[100:103]\n\t"
"ds_read_b128 v[120:123], v36 offset:45056\n\t"
"s_waitcnt vmcnt(24)\n\t"
"v_mfma_f32_16x16x32_bf16 v[64:67], v[104:107], v[148:151], v[64:67]\n\t"
"v_add_f32 v54, v54, v60\n\t"
"v_add_f32 v56, v56, v60\n\t"
"v_mul_f32 v58, v48, v48\n\t"
"v_fmac_f32 v58, v50, v50\n\t"
"v_mfma_f32_16x16x32_bf16 v[68:71], v[104:107], v[152:155], v[68:71]\n\t"
"ds_read_b128 v[104:107], v36 offset:51200\n\t"
"s_waitcnt lgkmcnt(3)\n\t"
"v_mfma_f32_16x16x32_bf16 v[72:75], v[108:111], v[148:151], v[72:75]\n\t"
"s_add_u32 s44, s36, 53248\n\t"
"buffer_load_dwordx4 v[140:143], v34, %[rs], s44 offen sc1\n\t"
"v_mfma_f32_16x16x32_bf16 v[76:79], v[108:111], v[152:155], v[76:79]\n\t"
"ds_read_b128 v[108:111], v36 offset:52224\n\t"
"v_mfma_f32_16x16x32_bf16 v[80:83], v[112:115], v[148:151], v[80:83]\n\t"
"v_fmac_f32 v58, v52, v52\n\t"
"v_fmac_f32 v58, v54, v54\n\t"
"v_fmac_f32 v58, v56, v56\n\t"
"v_mul_f32 v58, 0x42000000, v58\n\t"
"v_mfma_f32_16x16x32_bf16 v[84:87], v[112:115], v[152:155], v[84:87]\n\t"
"ds_read_b128 v[112:115], v36 offset:53248\n\t"
"s_waitcnt lgkmcnt(3)\n\t"
"v_mfma_f32_16x16x32_bf16 v[88:91], v[116:119], v[148:151], v[88:91]\n\t"
"buffer_load_dwordx4 v[144:147], v35, %[rs], s44 offen sc1\n\t"
"v_mfma_f32_16x16x32_bf16 v[92:95], v[116:119], v[152:155], v[92:95]\n\t"
"ds_read_b128 v[116:119], v36 offset:54272\n\t"
"v_mfma_f32_16x16x32_bf16 v[96:99], v[120:123], v[148:151], v[96:99]\n\t"
"v_add_f32 v58, v58, v49\n\t"
"v_add_f32 v58, v58, v51\n\t"
"v_add_f32 v58, v58, v53\n\t"
"v_add_f32 v58, v58, v55\n\t"
"v_add_f32 v58, v58, v57\n\t"
"v_mfma_f32_16x16x32_bf16 v[100:103], v[120:123], v[152:155], v[100:103]\n\t"
"ds_read_b128 v[120:123], v36 offset:55296\n\t"
"s_waitcnt vmcnt(21) lgkmcnt(3)\n\t"
"v_mfma_f32_16x16x32_bf16 v[64:67], v[104:107], v[156:159], v[64:67]\n\t"
"s_waitcnt vmcnt(20)\n\t"
"s_barrier\n\t"
"v_mfma_f32_16x16x32_bf16 v[68:71], v[104:107], v[160:163], v[68:71]\n\t"
"ds_read_b128 v[104:107], v37 offset:0\n\t"
"v_mfma_f32_16x16x32_bf16 v[72:75], v[108:111], v[156:159], v[72:75]\n\t"
"v_mov_b32 v59, v58\n\t"
"s_nop 1\n\t"
"v_permlane16_swap_b32 v58, v59\n\t"
"v_add_f32 v58, v58, v59\n\t"
"v_mfma_f32_16x16x32_bf16 v[76:79], v[108:111], v[160:163], v[76:79]\n\t"
"ds_read_b128 v[108:111], v37 offset:1024\n\t"
"s_waitcnt lgkmcnt(3)\n\t"
"v_mfma_f32_16x16x32_bf16 v[80:83], v[112:115], v[156:159], v[80:83]\n\t"
"v_mov_b32 v59, v58\n\t"
"s_nop 1\n\t"
"v_permlane32_swap_b32 v58, v59\n\t"
"v_add_f32 v58, v58, v59\n\t"
"v_mfma_f32_16x16x32_bf16 v[84:87], v[112:115], v[160:163], v[84:87]\n\t"
"ds_read_b128 v[112:115], v37 offset:2048\n\t"
"v_mfma_f32_16x16x32_bf16 v[88:91], v[116:119], v[156:159], v[88:91]\n\t"
"v_mov_b32 v59, 0x3727c5ac\n\t"
"v_fmac_f32 v59, 0x3acccccd, v58\n\t"
"v_rsq_f32 v58, v59\n\t"
"v_mfma_f32_16x16x32_bf16 v[92:95], v[116:119], v[160:163], v[92:95]\n\t"
"ds_read_b128 v[116:119], v37 offset:3072\n\t"
"s_waitcnt lgkmcnt(3)\n\t"
"v_mfma_f32_16x16x32_bf16 v[96:99], v[120:123], v[156:159], v[96:99]\n\t"
"v_lshlrev_b32 v200, 16, v248\n\t"
"v_and_b32 v201, 0xffff0000, v248\n\t"
"v_mfma_f32_16x16x32_bf16 v[100:103], v[120:123], v[160:163], v[100:103]\n\t"
"ds_read_b128 v[120:123], v37 offset:4096\n\t"
"s_waitcnt vmcnt(17)\n\t"
"v_mfma_f32_16x16x32_bf16 v[64:67], v[104:107], v[164:167], v[64:67]\n\t"
"v_lshlrev_b32 v202, 16, v249\n\t"
"v_and_b32 v203, 0xffff0000, v249\n\t"
"v_mfma_f32_16x16x32_bf16 v[68:71], v[104:107], v[168:171], v[68:71]\n\t"
"ds_read_b128 v[104:107], v37 offset:10240\n\t"
"s_waitcnt lgkmcnt(3)\n\t"
"v_mfma_f32_16x16x32_bf16 v[72:75], v[108:111], v[164:167], v[72:75]\n\t"
"v_lshlrev_b32 v204, 16, v250\n\t"
"v_and_b32 v205, 0xffff0000, v250\n\t"
"v_mfma_f32_16x16x32_bf16 v[76:79], v[108:111], v[168:171], v[76:79]\n\t"
"ds_read_b128 v[108:111], v37 offset:11264\n\t"
"v_mfma_f32_16x16x32_bf16 v[80:83], v[112:115], v[164:167], v[80:83]\n\t"
"v_lshlrev_b32 v206, 16, v251\n\t"
"v_and_b32 v207, 0xffff0000, v251\n\t"
"v_mfma_f32_16x16x32_bf16 v[84:87], v[112:115], v[168:171], v[84:87]\n\t"
"ds_read_b128 v[112:115], v37 offset:12288\n\t"
"s_waitcnt lgkmcnt(3)\n\t"
"v_mfma_f32_16x16x32_bf16 v[88:91], v[116:119], v[164:167], v[88:91]\n\t"
"v_lshlrev_b32 v208, 16, v188\n\t"
"v_and_b32 v209, 0xffff0000, v188\n\t"
"v_mfma_f32_16x16x32_bf16 v[92:95], v[116:119], v[168:171], v[92:95]\n\t"
"ds_read_b128 v[116:119], v37 offset:13312\n\t"
"v_mfma_f32_16x16x32_bf16 v[96:99], v[120:123], v[164:167], v[96:99]\n\t"
"v_lshlrev_b32 v210, 16, v189\n\t"
"v_and_b32 v211, 0xffff0000, v189\n\t"
"v_mfma_f32_16x16x32_bf16 v[100:103], v[120:123], v[168:171], v[100:103]\n\t"
"ds_read_b128 v[120:123], v37 offset:14336\n\t"
"s_waitcnt vmcnt(12) lgkmcnt(3)\n\t"
"v_mfma_f32_16x16x32_bf16 v[64:67], v[104:107], v[172:175], v[64:67]\n\t"
"v_lshlrev_b32 v212, 16, v190\n\t"
"v_and_b32 v213, 0xffff0000, v190\n\t"
"v_mfma_f32_16x16x32_bf16 v[68:71], v[104:107], v[176:179], v[68:71]\n\t"
"ds_read_b128 v[104:107], v37 offset:20480\n\t"
"v_mfma_f32_16x16x32_bf16 v[72:75], v[108:111], v[172:175], v[72:75]\n\t"
"v_lshlrev_b32 v214, 16, v191\n\t"
"v_and_b32 v215, 0xffff0000, v191\n\t"
"v_mfma_f32_16x16x32_bf16 v[76:79], v[108:111], v[176:179], v[76:79]\n\t"
"ds_read_b128 v[108:111], v37 offset:21504\n\t"
"s_waitcnt lgkmcnt(3)\n\t"
"v_mfma_f32_16x16x32_bf16 v[80:83], v[112:115], v[172:175], v[80:83]\n\t"
"v_lshlrev_b32 v216, 16, v252\n\t"
"v_and_b32 v217, 0xffff0000, v252\n\t"
"v_mfma_f32_16x16x32_bf16 v[84:87], v[112:115], v[176:179], v[84:87]\n\t"
"ds_read_b128 v[112:115], v37 offset:22528\n\t"
"v_mfma_f32_16x16x32_bf16 v[88:91], v[116:119], v[172:175], v[88:91]\n\t"
"v_lshlrev_b32 v218, 16, v253\n\t"
"v_and_b32 v219, 0xffff0000, v253\n\t"
"v_mfma_f32_16x16x32_bf16 v[92:95], v[116:119], v[176:179], v[92:95]\n\t"
"ds_read_b128 v[116:119], v37 offset:23552\n\t"
"s_waitcnt lgkmcnt(3)\n\t"
"v_mfma_f32_16x16x32_bf16 v[96:99], v[120:123], v[172:175], v[96:99]\n\t"
"v_mfma_f32_16x16x32_bf16 v[100:103], v[120:123], v[176:179], v[100:103]\n\t"
"ds_read_b128 v[120:123], v37 offset:24576\n\t"
"s_waitcnt vmcnt(7)\n\t"
"v_mfma_f32_16x16x32_bf16 v[64:67], v[104:107], v[180:183], v[64:67]\n\t"
"s_waitcnt vmcnt(6)\n\t"
"s_barrier\n\t"
"v_mfma_f32_16x16x32_bf16 v[68:71], v[104:107], v[184:187], v[68:71]\n\t"
"ds_read_b128 v[104:107], v37 offset:30720\n\t"
"s_waitcnt lgkmcnt(3)\n\t"
"v_mfma_f32_16x16x32_bf16 v[72:75], v[108:111], v[180:183], v[72:75]\n\t"
"v_mfma_f32_16x16x32_bf16 v[76:79], v[108:111], v[184:187], v[76:79]\n\t"
"ds_read_b128 v[108:111], v37 offset:31744\n\t"
"v_mfma_f32_16x16x32_bf16 v[80:83], v[112:115], v[180:183], v[80:83]\n\t"
"v_mfma_f32_16x16x32_bf16 v[84:87], v[112:115], v[184:187], v[84:87]\n\t"
"ds_read_b128 v[112:115], v37 offset:32768\n\t"
"s_waitcnt lgkmcnt(3)\n\t"
"v_mfma_f32_16x16x32_bf16 v[88:91], v[116:119], v[180:183], v[88:91]\n\t"
"v_mfma_f32_16x16x32_bf16 v[92:95], v[116:119], v[184:187], v[92:95]\n\t"
"ds_read_b128 v[116:119], v37 offset:33792\n\t"
"v_mfma_f32_16x16x32_bf16 v[96:99], v[120:123], v[180:183], v[96:99]\n\t"
"v_mfma_f32_16x16x32_bf16 v[100:103], v[120:123], v[184:187], v[100:103]\n\t"
"ds_read_b128 v[120:123], v37 offset:34816\n\t"
"s_waitcnt vmcnt(4) lgkmcnt(3)\n\t"
"v_mfma_f32_16x16x32_bf16 v[64:67], v[104:107], v[124:127], v[64:67]\n\t"
"v_mfma_f32_16x16x32_bf16 v[68:71], v[104:107], v[128:131], v[68:71]\n\t"
"ds_read_b128 v[104:107], v37 offset:40960\n\t"
"v_mfma_f32_16x16x32_bf16 v[72:75], v[108:111], v[124:127], v[72:75]\n\t"
"v_mfma_f32_16x16x32_bf16 v[76:79], v[108:111], v[128:131], v[76:79]\n\t"
"ds_read_b128 v[108:111], v37 offset:41984\n\t"
"s_waitcnt lgkmcnt(3)\n\t"
"v_mfma_f32_16x16x32_bf16 v[80:83], v[112:115], v[124:127], v[80:83]\n\t"
"v_mfma_f32_16x16x32_bf16 v[84:87], v[112:115], v[128:131], v[84:87]\n\t"
"ds_read_b128 v[112:115], v37 offset:43008\n\t"
"v_mfma_f32_16x16x32_bf16 v[88:91], v[116:119], v[124:127], v[88:91]\n\t"
"v_mfma_f32_16x16x32_bf16 v[92:95], v[116:119], v[128:131], v[92:95]\n\t"
"ds_read_b128 v[116:119], v37 offset:44032\n\t"
"s_waitcnt lgkmcnt(3)\n\t"
"v_mfma_f32_16x16x32_bf16 v[96:99], v[120:123], v[124:127], v[96:99]\n\t"
"v_mfma_f32_16x16x32_bf16 v[100:103], v[120:123], v[128:131], v[100:103]\n\t"
"ds_read_b128 v[120:123], v37 offset:45056\n\t"
"s_waitcnt vmcnt(2)\n\t"
"v_mfma_f32_16x16x32_bf16 v[64:67], v[104:107], v[132:135], v[64:67]\n\t"
"v_mfma_f32_16x16x32_bf16 v[68:71], v[104:107], v[136:139], v[68:71]\n\t"
"ds_read_b128 v[104:107], v37 offset:51200\n\t"
"s_waitcnt lgkmcnt(3)\n\t"
"v_mfma_f32_16x16x32_bf16 v[72:75], v[108:111], v[132:135], v[72:75]\n\t"
"v_mfma_f32_16x16x32_bf16 v[76:79], v[108:111], v[136:139], v[76:79]\n\t"
"ds_read_b128 v[108:111], v37 offset:52224\n\t"
"v_mfma_f32_16x16x32_bf16 v[80:83], v[112:115], v[132:135], v[80:83]\n\t"
"v_mfma_f32_16x16x32_bf16 v[84:87], v[112:115], v[136:139], v[84:87]\n\t"
"ds_read_b128 v[112:115], v37 offset:53248\n\t"
"s_waitcnt lgkmcnt(3)\n\t"
"v_mfma_f32_16x16x32_bf16 v[88:91], v[116:119], v[132:135], v[88:91]\n\t"
"v_mfma_f32_16x16x32_bf16 v[92:95], v[116:119], v[136:139], v[92:95]\n\t"
"ds_read_b128 v[116:119], v37 offset:54272\n\t"
"v_mfma_f32_16x16x32_bf16 v[96:99], v[120:123], v[132:135], v[96:99]\n\t"
"v_mfma_f32_16x16x32_bf16 v[100:103], v[120:123], v[136:139], v[100:103]\n\t"
"ds_read_b128 v[120:123], v37 offset:55296\n\t"
"s_waitcnt vmcnt(0) lgkmcnt(4)\n\t"
"v_mfma_f32_16x16x32_bf16 v[68:71], v[104:107], v[144:147], v[68:71]\n\t"
"s_waitcnt lgkmcnt(3)\n\t"
"v_mfma_f32_16x16x32_bf16 v[76:79], v[108:111], v[144:147], v[76:79]\n\t"
"s_waitcnt lgkmcnt(2)\n\t"
"v_mfma_f32_16x16x32_bf16 v[84:87], v[112:115], v[144:147], v[84:87]\n\t"
"s_waitcnt lgkmcnt(1)\n\t"
"v_mfma_f32_16x16x32_bf16 v[92:95], v[116:119], v[144:147], v[92:95]\n\t"
"s_waitcnt lgkmcnt(0)\n\t"
"v_mfma_f32_16x16x32_bf16 v[100:103], v[120:123], v[144:147], v[100:103]\n\t"
"v_mfma_f32_16x16x32_bf16 v[64:67], v[104:107], v[140:143], v[64:67]\n\t"
"v_mfma_f32_16x16x32_bf16 v[72:75], v[108:111], v[140:143], v[72:75]\n\t"
"v_mfma_f32_16x16x32_bf16 v[80:83], v[112:115], v[140:143], v[80:83]\n\t"
"s_nop 0\n\t"
"ds_write_b128 v40, v[68:71] offset:0\n\t"
"v_mfma_f32_16x16x32_bf16 v[88:91], v[116:119], v[140:143], v[88:91]\n\t"
"ds_write_b128 v40, v[76:79] offset:1024\n\t"
"v_mfma_f32_16x16x32_bf16 v[96:99], v[120:123], v[140:143], v[96:99]\n\t"
"ds_write_b128 v40, v[84:87] offset:2048\n\t"
"s_nop 0\n\t"
"ds_write_b128 v40, v[92:95] offset:3072\n\t"
"ds_write_b128 v40, v[100:103] offset:4096\n\t"
"s_waitcnt lgkmcnt(0)\n\t"
"s_barrier\n\t"
"ds_read_b128 v[104:107], v41 offset:0\n\t"
"ds_read_b128 v[108:111], v41 offset:1024\n\t"
"ds_read_b128 v[112:115], v41 offset:2048\n\t"
"ds_read_b128 v[116:119], v41 offset:3072\n\t"
"ds_read_b128 v[120:123], v41 offset:4096\n\t"
"s_waitcnt lgkmcnt(4)\n\t"
"v_pk_add_f32 v[64:65], v[64:65], v[104:105]\n\t"
"v_pk_add_f32 v[66:67], v[66:67], v[106:107]\n\t"
"v_pk_fma_f32 v[200:201], v[64:65], v[58:59], v[200:201] op_sel_hi:[1,0,1]\n\t"
"v_pk_fma_f32 v[202:203], v[66:67], v[58:59], v[202:203] op_sel_hi:[1,0,1]\n\t"
"v_cvt_pk_bf16_f32 v124, v200, v201\n\t"
"v_cvt_pk_bf16_f32 v125, v202, v203\n\t"
"s_waitcnt lgkmcnt(3)\n\t"
"v_pk_add_f32 v[72:73], v[72:73], v[108:109]\n\t"
"v_pk_add_f32 v[74:75], v[74:75], v[110:111]\n\t"
"v_pk_fma_f32 v[204:205], v[72:73], v[58:59], v[204:205] op_sel_hi:[1,0,1]\n\t"
"v_pk_fma_f32 v[206:207], v[74:75], v[58:59], v[206:207] op_sel_hi:[1,0,1]\n\t"
"v_cvt_pk_bf16_f32 v126, v204, v205\n\t"
"v_cvt_pk_bf16_f32 v127, v206, v207\n\t"
"buffer_store_dwordx4 v[124:127], v42, %[ro], s40 offen sc1\n\t"
"s_waitcnt lgkmcnt(2)\n\t"
"v_pk_add_f32 v[80:81], v[80:81], v[112:113]\n\t"
"v_pk_add_f32 v[82:83], v[82:83], v[114:115]\n\t"
"v_pk_fma_f32 v[208:209], v[80:81], v[58:59], v[208:209] op_sel_hi:[1,0,1]\n\t"
"v_pk_fma_f32 v[210:211], v[82:83], v[58:59], v[210:211] op_sel_hi:[1,0,1]\n\t"
"v_cvt_pk_bf16_f32 v128, v208, v209\n\t"
"v_cvt_pk_bf16_f32 v129, v210, v211\n\t"
"s_waitcnt lgkmcnt(1)\n\t"
"v_pk_add_f32 v[88:89], v[88:89], v[116:117]\n\t"
"v_pk_add_f32 v[90:91], v[90:91], v[118:119]\n\t"
"v_pk_fma_f32 v[212:213], v[88:89], v[58:59], v[212:213] op_sel_hi:[1,0,1]\n\t"
"v_pk_fma_f32 v[214:215], v[90:91], v[58:59], v[214:215] op_sel_hi:[1,0,1]\n\t"
"v_cvt_pk_bf16_f32 v130, v212, v213\n\t"
"v_cvt_pk_bf16_f32 v131, v214, v215\n\t"
"buffer_store_dwordx4 v[128:131], v43, %[ro], s40 offen sc1\n\t"
"s_waitcnt lgkmcnt(0)\n\t"
"v_pk_add_f32 v[96:97], v[96:97], v[120:121]\n\t"
"v_pk_add_f32 v[98:99], v[98:99], v[122:123]\n\t"
"v_pk_fma_f32 v[216:217], v[96:97], v[58:59], v[216:217] op_sel_hi:[1,0,1]\n\t"
"v_pk_fma_f32 v[218:219], v[98:99], v[58:59], v[218:219] op_sel_hi:[1,0,1]\n\t"
"v_cvt_pk_bf16_f32 v132, v216, v217\n\t"
"v_cvt_pk_bf16_f32 v133, v218, v219\n\t"
"buffer_store_dwordx2 v[132:133], v44, %[ro], s40 offen sc1\n\t"
"s_cmp_lg_u32 s41, 0\n\t"
"s_cbranch_scc1 L_b2_end_%=\n\t"
"v_pk_add_f32 v[64:65], v[200:201], v[202:203]\n\t"
"v_pk_add_f32 v[66:67], v[204:205], v[206:207]\n\t"
"v_pk_add_f32 v[64:65], v[64:65], v[208:209]\n\t"
"v_pk_add_f32 v[66:67], v[66:67], v[210:211]\n\t"
"v_pk_add_f32 v[64:65], v[64:65], v[212:213]\n\t"
"v_pk_add_f32 v[66:67], v[66:67], v[214:215]\n\t"
"v_pk_add_f32 v[64:65], v[64:65], v[216:217]\n\t"
"v_pk_add_f32 v[66:67], v[66:67], v[218:219]\n\t"
"v_pk_add_f32 v[64:65], v[64:65], v[66:67]\n\t"
"v_add_f32 v64, v64, v65\n\t"
"v_mov_b32 v68, v64\n\t"
"s_nop 1\n\t"
"v_permlane16_swap_b32 v64, v68\n\t"
"v_add_f32 v64, v64, v68\n\t"
"v_mov_b32 v68, v64\n\t"
"s_nop 1\n\t"
"v_permlane32_swap_b32 v64, v68\n\t"
"v_add_f32 v64, v64, v68\n\t"
"v_mul_f32 v64, 0x3c4ccccd, v64\n\t"
"v_pk_add_f32 v[200:201], v[200:201], v[64:65] op_sel_hi:[1,0] neg_lo:[0,1] neg_hi:[0,1]\n\t"
"v_pk_add_f32 v[202:203], v[202:203], v[64:65] op_sel_hi:[1,0] neg_lo:[0,1] neg_hi:[0,1]\n\t"
"v_pk_add_f32 v[204:205], v[204:205], v[64:65] op_sel_hi:[1,0] neg_lo:[0,1] neg_hi:[0,1]\n\t"
"v_pk_add_f32 v[206:207], v[206:207], v[64:65] op_sel_hi:[1,0] neg_lo:[0,1] neg_hi:[0,1]\n\t"
"v_pk_add_f32 v[208:209], v[208:209], v[64:65] op_sel_hi:[1,0] neg_lo:[0,1] neg_hi:[0,1]\n\t"
"v_pk_add_f32 v[210:211], v[210:211], v[64:65] op_sel_hi:[1,0] neg_lo:[0,1] neg_hi:[0,1]\n\t"
"v_pk_add_f32 v[212:213], v[212:213], v[64:65] op_sel_hi:[1,0] neg_lo:[0,1] neg_hi:[0,1]\n\t"
"v_pk_add_f32 v[214:215], v[214:215], v[64:65] op_sel_hi:[1,0] neg_lo:[0,1] neg_hi:[0,1]\n\t"
"v_pk_add_f32 v[216:217], v[216:217], v[64:65] op_sel_hi:[1,0] neg_lo:[0,1] neg_hi:[0,1]\n\t"
"v_pk_add_f32 v[218:219], v[218:219], v[64:65] op_sel_hi:[1,0] neg_lo:[0,1] neg_hi:[0,1]\n\t"
"v_pk_mul_f32 v[66:67], v[200:201], v[200:201]\n\t"
"v_pk_mul_f32 v[68:69], v[202:203], v[202:203]\n\t"
"v_pk_fma_f32 v[66:67], v[204:205], v[204:205], v[66:67]\n\t"
"v_pk_fma_f32 v[68:69], v[206:207], v[206:207], v[68:69]\n\t"
"v_pk_fma_f32 v[66:67], v[208:209], v[208:209], v[66:67]\n\t"
"v_pk_fma_f32 v[68:69], v[210:211], v[210:211], v[68:69]\n\t"
"v_pk_fma_f32 v[66:67], v[212:213], v[212:213], v[66:67]\n\t"
"v_pk_fma_f32 v[68:69], v[214:215], v[214:215], v[68:69]\n\t"
"v_pk_fma_f32 v[66:67], v[216:217], v[216:217], v[66:67]\n\t"
"v_pk_fma_f32 v[68:69], v[218:219], v[218:219], v[68:69]\n\t"
"v_pk_add_f32 v[66:67], v[66:67], v[68:69]\n\t"
"v_add_f32 v65, v66, v67\n\t"
"v_mov_b32 v68, v65\n\t"
"s_nop 1\n\t"
"v_permlane16_swap_b32 v65, v68\n\t"
"v_add_f32 v65, v65, v68\n\t"
"v_mov_b32 v68, v65\n\t"
"s_nop 1\n\t"
"v_permlane32_swap_b32 v65, v68\n\t"
"v_add_f32 v65, v65, v68\n\t"
"buffer_store_dwordx2 v[64:65], v45, %[rs], s42 offen sc1\n\t"
"s_waitcnt vmcnt(0)\n\t"
"buffer_store_dwordx2 v[32:33], v46, %[rs], s43 offen sc1\n\t"
"L_b2_end_%=:\n\t"

                   : "+{s[36:39]}"(b2_S0), "+{s[40:43]}"(b2_S1), "+{v[32:35]}"(b2_V0), "+{v[36:39]}"(b2_V1), "+{v[40:43]}"(b2_V2), "+{v[44:47]}"(b2_V3), "+{v[48:55]}"(s8), "+{v[56:57]}"(s2), "+{v[248:251]}"(ra0), "+{v[188:191]}"(ra1), "+{v[252:253]}"(ra2),
                     "+{v[200:215]}"(g0), "+{v[216:231]}"(g1), "+{v[232:247]}"(g2)
                   : [rs] "s"(rs), [ro] "s"(rO)
                   : "memory", "scc", "s44","s45","v64","v65","v66","v67","v68","v69","v70","v71","v72","v73","v74","v75","v76","v77","v78","v79","v80","v81","v82","v83","v84","v85","v86","v87","v88","v89","v90","v91","v92","v93","v94","v95","v96","v97","v98","v99","v100","v101","v102","v103","v104","v105","v106","v107","v108","v109","v110","v111","v112","v113","v114","v115","v116","v117","v118","v119","v120","v121","v122","v123","v124","v125","v126","v127","v128","v129","v130","v131","v132","v133","v134","v135","v136","v137","v138","v139","v140","v141","v142","v143","v144","v145","v146","v147","v148","v149","v150","v151","v152","v153","v154","v155","v156","v157","v158","v159","v160","v161","v162","v163","v164","v165","v166","v167","v168","v169","v170","v171","v172","v173","v174","v175","v176","v177","v178","v179","v180","v181","v182","v183","v184","v185","v186","v187","v58","v59","v60", ACLOB);
      if (ly == 0) {
        WD_VARS();
        wd_groups(0, 5, w, 5 * (c16 & 7), wdv, rWd);
      } else {
        if (bid == 0 && tid == 0) __builtin_amdgcn_raw_buffer_store_b64(ev, rs, 0, OFF_E, SC1);
      }
    }
  }
}

extern "C" void kernel_launch(void* const* d_in, const int* in_sizes, int n_in, void* d_out, int out_size, void* d_ws, size_t ws_size,
                              hipStream_t stream) {
  Params p;
  p.x0 = (const u16*)d_in[0]; p.cre = (const u16*)d_in[1]; p.cim = (const u16*)d_in[2];
  p.wre = (const u16*)d_in[3]; p.wim = (const u16*)d_in[4]; p.w1 = (const u16*)d_in[6]; p.w2 = (const u16*)d_in[7];
  p.out = (u16*)d_out; p.ws = (char*)d_ws;
  (void)hipFuncSetAttribute((const void*)fnet_fused, hipFuncAttributeMaxDynamicSharedMemorySize, LDS_BYTES);
  fnet_fused<<<256, 256, LDS_BYTES, stream>>>(p);
}
```
